```python
import torch

def _softmax(x, axis=-1):
    return torch.softmax(x, dim=axis)
def _gelu(x):
    return torch.nn.functional.gelu(x, approximate="tanh")
def _silu(x):
    return torch.nn.functional.silu(x)
def _sigmoid(x):
    return torch.sigmoid(x)
def _layernorm(x, axis=-1, eps=1e-5):
    m = x.mean(dim=axis, keepdim=True)
    v = x.var(dim=axis, keepdim=True, unbiased=False)
    return (x - m) / torch.sqrt(v + eps)
def _scatter_add(tgt, idx, src, axis=0):
    return tgt.clone().index_add_(axis, idx, src)
def _segment_sum(src, seg, n):
    out = torch.zeros((n,) + src.shape[1:], dtype=src.dtype)
    return out.index_add_(0, seg, src)
def _pad(x, pw):
    flat = tuple(v for pair in reversed(pw) for v in pair)
    return torch.nn.functional.pad(x, flat)

kernel_name = 'sequential_preLN__sliding_block__moe_arch_mod__prompt'

def setup_inputs():

    _w = (lambda a: (lambda t: t.to(torch.bfloat16) if t.is_floating_point() else t)(torch.as_tensor(a)))
    def _A_setup():
        B, S, D = 1, 2048, 512
        return {
            "x": _w((lambda *s: torch.randn(*s, dtype=torch.bfloat16) * 0.05)(B, S, D)),
            "wqkv": _w((lambda *s: torch.randn(*s, dtype=torch.bfloat16) * 0.05)(D, 3 * D)),
            "wo": _w((lambda *s: torch.randn(*s, dtype=torch.bfloat16) * 0.05)(D, D)),
        }
    _A_in = _A_setup()

    def _B_setup():
        B, S, D, F, C = 1, 2048, 512, 1792, 1
        return {
            "x": _w((lambda *s: torch.randn(*s, dtype=torch.bfloat16) * 0.05)(B, S, D)),
            "w_r": _w((lambda *s: torch.randn(*s, dtype=torch.bfloat16) * 0.05)(D)),
            "w_gate": _w((lambda *s: torch.randn(*s, dtype=torch.bfloat16) * 0.05)(D, F)),
            "w_up": _w((lambda *s: torch.randn(*s, dtype=torch.bfloat16) * 0.05)(D, F)),
            "w_down": _w((lambda *s: torch.randn(*s, dtype=torch.bfloat16) * 0.05)(F, D)),
        }
    _B_in = _B_setup()

    return {
        "A_x": _A_in["x"],
        "A_wqkv": _A_in["wqkv"],
        "A_wo": _A_in["wo"],
        "B_x": _B_in["x"],
        "B_w_r": _B_in["w_r"],
        "B_w_gate": _B_in["w_gate"],
        "B_w_up": _B_in["w_up"],
        "B_w_down": _B_in["w_down"],
    }

def reference(A_x, A_wqkv, A_wo, B_x, B_w_r, B_w_gate, B_w_up, B_w_down):
    def _A_ref(x, wqkv, wo):
        B, S, D = x.shape
        H = 32; Dh = D // H; W = 256
        qkv = (lambda x, *d: x.permute(*d))((x @ wqkv).reshape(B, S, 3, H, Dh), 2, 0, 3, 1, 4)
        q, k, v = qkv[0], qkv[1], qkv[2]
        scores = torch.einsum("bhqd,bhkd->bhqk", q, k) * (Dh ** -0.5)
        idx = torch.arange(S)
        mask = (idx[:, None] - idx[None, :] >= 0) & (idx[:, None] - idx[None, :] < W)
        scores = torch.where(mask[None, None], scores, -1e9)
        p = _softmax(scores, axis=-1)
        o = torch.einsum("bhqk,bhkd->bhqd", p, v)
        return (lambda x, *d: x.permute(*d))(o, 0, 2, 1, 3).reshape(B, S, D) @ wo

    def _B_ref(x, w_r, w_gate, w_up, w_down):
        B, S, D = x.shape
        C = 1
        r = x @ w_r
        idx = torch.argsort(-r, axis=-1)[:, :C]
        idx_e = (lambda x, n, axis: torch.repeat_interleave(x, n, dim=axis))(idx.reshape(B, C, 1), D, axis=2)
        x_sel = (lambda x, i, axis: torch.take_along_dim(x, i, dim=axis))(x, idx_e, axis=1)
        g = _silu(x_sel @ w_gate)
        u = x_sel @ w_up
        y = (g * u) @ w_down
        r_sel = _sigmoid((lambda x, i, axis: torch.take_along_dim(x, i, dim=axis))(r, idx, axis=1))
        y = y * r_sel.reshape(B, C, 1)
        b_off = (torch.arange(B) * S).reshape(B, 1)
        flat_idx = (idx + b_off).reshape(B * C)
        zero = (x * 0.0).reshape(B * S, D)
        upd = _scatter_add(zero, flat_idx, y.reshape(B * C, D), axis=0)
        return x + upd.reshape(B, S, D)

    x = A_x
    h = x + _A_ref(_layernorm(x, axis=-1), A_wqkv, A_wo)
    h2 = h + _B_ref(_layernorm(h, axis=-1), B_w_r, B_w_gate, B_w_up, B_w_down)
    return h2
```

```cpp
#include <hip/hip_runtime.h>
#include <hip/hip_bf16.h>
#include <stdint.h>
typedef unsigned short u16;
__device__ __forceinline__ float bf2f(u16 b){ return __uint_as_float(((unsigned)b)<<16); }
__device__ __forceinline__ u16 f2bf(float f){ unsigned u=__float_as_uint(f); u += 0x7fffu + ((u>>16)&1u); return (u16)(u>>16); }
constexpr int S=2048,D=512,F=1792,H=32,DH=16,W=256;

__global__ void ln_kernel(const u16* x, u16* y){
  int row=blockIdx.x, lane=threadIdx.x;
  float v[8]; float s=0;
  for(int i=0;i<8;i++){ v[i]=bf2f(x[row*D+lane*8+i]); s+=v[i]; }
  for(int m=32;m>=1;m>>=1) s+=__shfl_xor(s,m);
  float mean=s/D; float q=0;
  for(int i=0;i<8;i++){ float d=v[i]-mean; q+=d*d; }
  for(int m=32;m>=1;m>>=1) q+=__shfl_xor(q,m);
  float rstd=rsqrtf(q/D+1e-5f);
  for(int i=0;i<8;i++) y[row*D+lane*8+i]=f2bf((v[i]-mean)*rstd);
}
__global__ void gemm_kernel(const u16* A, const u16* B, const u16* res, u16* C, int N, int K){
  int col=blockIdx.x*blockDim.x+threadIdx.x, row=blockIdx.y;
  float acc=0;
  for(int k=0;k<K;k++) acc+=bf2f(A[row*K+k])*bf2f(B[k*N+col]);
  if(res) acc=bf2f(f2bf(acc))+bf2f(res[row*N+col]);
  C[row*N+col]=f2bf(acc);
}
__global__ void attn_kernel(const u16* qkv, u16* o){
  int q=blockIdx.x*blockDim.x+threadIdx.x, h=blockIdx.y;
  float qv[DH]; for(int d=0;d<DH;d++) qv[d]=bf2f(qkv[q*3*D+h*DH+d]);
  int k0=q-W+1; if(k0<0)k0=0;
  float m=-1e30f;
  for(int k=k0;k<=q;k++){ float s=0; for(int d=0;d<DH;d++) s+=qv[d]*bf2f(qkv[k*3*D+D+h*DH+d]); s*=0.25f; m=fmaxf(m,s);}
  float l=0; float acc[DH]; for(int d=0;d<DH;d++) acc[d]=0;
  for(int k=k0;k<=q;k++){ float s=0; for(int d=0;d<DH;d++) s+=qv[d]*bf2f(qkv[k*3*D+D+h*DH+d]); s*=0.25f; float p=__expf(s-m); l+=p; for(int d=0;d<DH;d++) acc[d]+=p*bf2f(qkv[k*3*D+2*D+h*DH+d]); }
  for(int d=0;d<DH;d++) o[q*D+h*DH+d]=f2bf(acc[d]/l);
}
__global__ void ln2_kernel(const u16* hh, const u16* wr, u16* y, float* r, u16* out){
  int row=blockIdx.x, lane=threadIdx.x;
  float v[8]; float s=0;
  for(int i=0;i<8;i++){ v[i]=bf2f(hh[row*D+lane*8+i]); s+=v[i]; }
  for(int m=32;m>=1;m>>=1) s+=__shfl_xor(s,m);
  float mean=s/D; float q=0;
  for(int i=0;i<8;i++){ float d=v[i]-mean; q+=d*d; }
  for(int m=32;m>=1;m>>=1) q+=__shfl_xor(q,m);
  float rstd=rsqrtf(q/D+1e-5f); float dot=0;
  for(int i=0;i<8;i++){ u16 t=f2bf((v[i]-mean)*rstd); y[row*D+lane*8+i]=t; dot+=bf2f(t)*bf2f(wr[lane*8+i]); out[row*D+lane*8+i]=f2bf(v[i]+bf2f(t)); }
  for(int m=32;m>=1;m>>=1) dot+=__shfl_xor(dot,m);
  if(lane==0) r[row]=dot;
}
__device__ int argmax_block(const float* r, float* sv, int* si){
  int t=threadIdx.x; float best=-1e30f; int bi=0;
  for(int i=t;i<S;i+=blockDim.x){ float v=r[i]; if(v>best){best=v;bi=i;} }
  sv[t]=best; si[t]=bi; __syncthreads();
  if(t==0){ for(int i=1;i<(int)blockDim.x;i++){ if(sv[i]>sv[0]||(sv[i]==sv[0]&&si[i]<si[0])){sv[0]=sv[i];si[0]=si[i];} } }
  __syncthreads(); return si[0];
}
__global__ void ffn1_kernel(const float* r, const u16* xn, const u16* wg, const u16* wu, float* gu){
  __shared__ float sv[64]; __shared__ int si[64];
  int idx=argmax_block(r,sv,si);
  int f=blockIdx.x*64+threadIdx.x; float g=0,u=0;
  for(int k=0;k<D;k++){ float xv=bf2f(xn[idx*D+k]); g+=xv*bf2f(wg[k*F+f]); u+=xv*bf2f(wu[k*F+f]); }
  g=g/(1.f+__expf(-g)); gu[f]=g*u;
}
__global__ void ffn2_kernel(const float* r, const u16* xn, const u16* hh, const u16* wd, const float* gu, u16* out){
  __shared__ float sv[64]; __shared__ int si[64];
  int idx=argmax_block(r,sv,si);
  int c=blockIdx.x*64+threadIdx.x; float y=0;
  for(int k=0;k<F;k++) y+=gu[k]*bf2f(wd[k*D+c]);
  float sg=1.f/(1.f+__expf(-r[idx]));
  out[idx*D+c]=f2bf(bf2f(hh[idx*D+c])+bf2f(xn[idx*D+c])+y*sg);
}
extern "C" void kernel_launch(void* const* d_in, const int* in_sizes, int n_in, void* d_out, int out_size, void* d_ws, size_t ws_size, hipStream_t stream){
  const u16* x=(const u16*)d_in[0]; const u16* wqkv=(const u16*)d_in[1]; const u16* wo=(const u16*)d_in[2];
  const u16* wr=(const u16*)d_in[4]; const u16* wg=(const u16*)d_in[5]; const u16* wu=(const u16*)d_in[6]; const u16* wd=(const u16*)d_in[7];
  u16* out=(u16*)d_out; char* ws=(char*)d_ws;
  u16* xn=(u16*)ws; u16* qkv=(u16*)(ws+(4<<20)); u16* o=(u16*)(ws+(12<<20)); u16* hh=(u16*)(ws+(16<<20)); u16* xn2=(u16*)(ws+(20<<20)); float* r=(float*)(ws+(24<<20)); float* gu=(float*)(ws+(25<<20));
  ln_kernel<<<S,64,0,stream>>>(x,xn);
  gemm_kernel<<<dim3(3*D/256,S),256,0,stream>>>(xn,wqkv,nullptr,qkv,3*D,D);
  attn_kernel<<<dim3(S/64,H),64,0,stream>>>(qkv,o);
  gemm_kernel<<<dim3(D/256,S),256,0,stream>>>(o,wo,x,hh,D,D);
  ln2_kernel<<<S,64,0,stream>>>(hh,wr,xn2,r,out);
  ffn1_kernel<<<F/64,64,0,stream>>>(r,xn2,wg,wu,gu);
  ffn2_kernel<<<D/64,64,0,stream>>>(r,xn2,hh,wd,gu,out);
}
```

```cpp
#include <hip/hip_runtime.h>
#include <stdint.h>

typedef unsigned u32;
typedef unsigned short u16;
typedef u32 u32x4 __attribute__((ext_vector_type(4)));
typedef u32 u32x2 __attribute__((ext_vector_type(2)));
typedef float f32x2 __attribute__((ext_vector_type(2)));
typedef float f32x4 __attribute__((ext_vector_type(4)));
typedef float f32x16 __attribute__((ext_vector_type(16)));
typedef short s16x4 __attribute__((ext_vector_type(4)));
typedef __bf16 bf16x2 __attribute__((ext_vector_type(2)));
typedef __bf16 bf16x8 __attribute__((ext_vector_type(8)));
typedef __amdgpu_buffer_rsrc_t srd_t;
#define LAS __attribute__((address_space(3)))
#define DI __device__ __forceinline__

#define STAMPS 0
#define FORCE_SLOW 0

constexpr int SEQ = 2048, DM = 512, FF = 1792;
constexpr u32 WS_KV = 4096, KV_UNIT = 2176;
constexpr u32 WS_O = 5u << 20;
constexpr u32 WS_OC = 7u << 20;
constexpr u32 WS_ST = WS_OC + 65536;
constexpr u32 WS_CAND = WS_ST + 262144;
constexpr u32 WS_F1 = WS_CAND + 65536;
constexpr u32 WS_F2 = WS_F1 + 262144;
constexpr u32 WS_END = WS_F2 + 131072;
constexpr u32 WS_DBG = 16u << 20;
constexpr u32 LX = 0;
constexpr u32 LR = 65536;
constexpr u32 LM = LR + 10 * 8192;
constexpr u32 L_KVX = LM;
constexpr u32 L_STA = LM + 8192, L_STB = L_STA + 256;
constexpr u32 L_WR = L_STB + 256;
constexpr u32 L_OCX = L_WR + 1024;
constexpr u32 L_KEY = L_OCX + 128;
constexpr u32 L_VEC = L_KEY + 256;
constexpr u32 L_EPO = L_VEC + 128;
constexpr u32 L_QX = LR + 4 * 8192;
constexpr u32 L_OX = LR;
constexpr u32 L_PX = LR + 5 * 8192;
constexpr u32 L_FW = LR + 7 * 8192;
constexpr u32 L_LN = LR;
constexpr u32 L_OUT = LR + 8192;
constexpr u32 LDS_BYTES = 163840;
constexpr u32 SPIN_CAP = 1u << 16;
constexpr u32 ONES = 0x3F803F80u;

DI u32 f2u(float f) { return __builtin_bit_cast(u32, f); }
DI float u2f(u32 u) { return __builtin_bit_cast(float, u); }
DI u32 pk(float lo, float hi) { f32x2 v = {lo, hi}; return __builtin_bit_cast(u32, __builtin_convertvector(v, bf16x2)); }
DI float bflo(u32 d) { return u2f(d << 16); }
DI float bfhi(u32 d) { return u2f(d & 0xffff0000u); }
DI f32x4 mfma16(u32x4 a, u32x4 b, f32x4 c) { return __builtin_amdgcn_mfma_f32_16x16x32_bf16(__builtin_bit_cast(bf16x8, a), __builtin_bit_cast(bf16x8, b), c, 0, 0, 0); }
DI f32x16 mfma32(u32x4 a, u32x4 b, f32x16 c) { return __builtin_amdgcn_mfma_f32_32x32x16_bf16(__builtin_bit_cast(bf16x8, a), __builtin_bit_cast(bf16x8, b), c, 0, 0, 0); }
template <class T> DI T ldsr(u32 a) { return *(const LAS T*)(size_t)a; }
template <class T> DI void ldsw(u32 a, T v) { *(LAS T*)(size_t)a = v; }
DI u32x2 ldstr(u32 a) { return __builtin_bit_cast(u32x2, __builtin_amdgcn_ds_read_tr16_b64_v4i16((LAS s16x4*)(size_t)a)); }
DI u32x4 trfrag(u32 a, u32 step) { u32x2 lo = ldstr(a), hi = ldstr(a + step); return (u32x4){lo.x, lo.y, hi.x, hi.y}; }
template <int CTRL> DI u32 dppu(u32 v) { return __builtin_amdgcn_mov_dpp(v, CTRL, 0xf, 0xf, true); }
template <int CTRL> DI float dppf(float v) { return u2f(dppu<CTRL>(f2u(v))); }
constexpr int DPP_X1 = 0xB1, DPP_X2 = 0x4E, DPP_HM = 0x141, DPP_R8 = 0x128;
DI void swap16(u32& a, u32& b) { auto r = __builtin_amdgcn_permlane16_swap(a, b, false, false); a = r[0]; b = r[1]; }
DI void swap32(u32& a, u32& b) { auto r = __builtin_amdgcn_permlane32_swap(a, b, false, false); a = r[0]; b = r[1]; }
DI u32 sum4(u32x4 v) { return v.x + v.y + v.z + v.w; }
DI u32 rowsum16(u32 v) { v += dppu<DPP_X1>(v); v += dppu<DPP_X2>(v); v += dppu<DPP_HM>(v); v += dppu<DPP_R8>(v); return v; }
DI float sum8f(float v) { v += dppf<DPP_X1>(v); v += dppf<DPP_X2>(v); v += dppf<DPP_HM>(v); return v; }
DI srd_t mksrd(const void* p, u32 bytes) { return __builtin_amdgcn_make_buffer_rsrc((void*)p, (short)0, (int)bytes, 0x00020000); }

#define BAR() asm volatile("s_waitcnt lgkmcnt(0)\n\ts_barrier" ::: "memory")
#define VMBAR(N) asm volatile("s_waitcnt vmcnt(" #N ") lgkmcnt(0)\n\ts_barrier" ::: "memory")
#define DMA(srd, voff, soff, ldst) asm volatile("s_mov_b32 m0, %0\n\ts_nop 0\n\tbuffer_load_dwordx4 %1, %2, %3 offen lds" ::"s"(ldst), "v"(voff), "s"(srd), "s"(soff) : "memory")
#define LD4(dst, srd, voff, soff, imm) asm volatile("buffer_load_dwordx4 %0, %1, %2, %3 offen offset:" #imm " sc1" : "=v"(dst) : "v"(voff), "s"(srd), "s"(soff) : "memory")
#define LD1(dst, srd, voff, soff, imm) asm volatile("buffer_load_dword %0, %1, %2, %3 offen offset:" #imm " sc1" : "=v"(dst) : "v"(voff), "s"(srd), "s"(soff) : "memory")
#define ST4(src, srd, voff, soff, imm) asm volatile("buffer_store_dwordx4 %0, %1, %2, %3 offen offset:" #imm " sc1\n\ts_nop 1" ::"v"(src), "v"(voff), "s"(srd), "s"(soff) : "memory")
#define ST2(src, srd, voff, soff, imm) asm volatile("buffer_store_dwordx2 %0, %1, %2, %3 offen offset:" #imm " sc1" ::"v"(src), "v"(voff), "s"(srd), "s"(soff) : "memory")
#define ST1(src, srd, voff, soff, imm) asm volatile("buffer_store_dword %0, %1, %2, %3 offen offset:" #imm " sc1" ::"v"(src), "v"(voff), "s"(srd), "s"(soff) : "memory")
#define WAIT3(N, a, b, c) asm volatile("s_waitcnt vmcnt(" #N ")" : "+v"(a), "+v"(b), "+v"(c)::"memory")

#define STAMP(i)

DI f32x16 att_tile(u32x4 K, u32x4 Q, u32x4 A0, u32x4 A1, f32x16 acc) {
  const f32x16 z = {};
  f32x16 s = mfma32(K, Q, z);
  u32x4 P0, P1;
#pragma unroll
  for (int i = 0; i < 4; ++i) {
    P0[i] = pk(__builtin_amdgcn_exp2f(s[2 * i]), __builtin_amdgcn_exp2f(s[2 * i + 1]));
    P1[i] = pk(__builtin_amdgcn_exp2f(s[8 + 2 * i]), __builtin_amdgcn_exp2f(s[9 + 2 * i]));
  }
  acc = mfma32(A0, P0, acc);
  return mfma32(A1, P1, acc);
}
DI void vexpand(u32x4 R, u32 ones, u32x4& A0, u32x4& A1) {
#pragma unroll
  for (int i = 0; i < 4; ++i) { u32 a = R[i], b = ones; swap16(a, b); A0[i] = a; A1[i] = b; }
}

__global__ __launch_bounds__(512, 2) void fused_block(const u16* __restrict__ x, const u16* __restrict__ wqkv, const u16* __restrict__ wo,
                                                      const u16* __restrict__ wr, const u16* __restrict__ wg, const u16* __restrict__ wu,
                                                      const u16* __restrict__ wd, u16* out, char* ws) {
  __shared__ __attribute__((aligned(16))) char lds_[LDS_BYTES];
  asm volatile("" ::"s"((u32)(size_t)lds_));
  STAMP(0);
  const u32 lane = threadIdx.x & 63;
  const u32 wave = __builtin_amdgcn_readfirstlane(threadIdx.x >> 6);
  const u32 b = blockIdx.x, xg = b & 7, bi = b >> 3;
  const u32 tb = (xg >> 1) * 8 + (bi & 7), hq = (xg & 1) * 4 + (bi >> 3);
  const u32 hd = wave & 3, p = wave >> 2;
  const u32 c = lane & 15, g = lane >> 4, rr = lane >> 3, s8 = lane & 7;
  const u32 r0 = (tb + 2 * hq) & 7;
  const srd_t srdS = mksrd(ws, WS_END);
  const u32 zero = 0;
  u32x4 my0, my1;
  u32 tag;
  const u32 xb0 = LX + c * 128 + ((g ^ (c >> 1)) * 16), xb1 = xb0 ^ 64;
  const u32 wsw = ((lane >> 3) & 1) | ((g & 1) << 1);
  const u32 wtr = (8 * g + ((lane >> 2) & 3)) * 128 + (lane & 3) * 8;
  const u32 wb = LR + wtr + ((hd ^ wsw) * 32);
  const u32x4 ones4 = {ONES, ONES, ONES, ONES};
  const u32 head = hq * 4 + hd;

  if (p == 1) {
    const srd_t srdX = mksrd((const char*)x + (size_t)tb * 65536, 65536);
    const srd_t srdW = mksrd((const char*)wqkv + hq * 128, DM * 3 * DM * 2 - hq * 128);
    const u32 vx0 = (16 * hd + rr) * 1024 + ((s8 ^ (rr >> 1)) * 16), vx1 = (vx0 + 8192) ^ 64;
    const u32 vw0 = (16 * hd + rr) * 3072 + ((s8 ^ (rr & 2)) * 16), vw1 = (vw0 + 8 * 3072) ^ 64;
    const u32 lw = hd * 2048;
#define DMAX(cc) do { const u32 so_ = (((cc) + r0) & 7) * 128; DMA(srdX, vx0, so_, lw + (LX + (cc) * 8192)); DMA(srdX, vx1, so_, lw + (LX + (cc) * 8192 + 1024)); } while (0)
#define DMAW(n, part, cc) do { const u32 so_ = (((cc) + r0) & 7) * (64 * 3072) + (part) * 1024; DMA(srdW, vw0, so_, lw + (LR + ((n) % 10) * 8192)); DMA(srdW, vw1, so_, lw + (LR + ((n) % 10) * 8192 + 1024)); } while (0)
    DMAX(0); DMAW(0, 1, 0); DMAW(1, 2, 0); DMAX(1);
    DMAW(2, 1, 1); DMAW(3, 2, 1); DMAX(2);
    float s1[2] = {0.f, 0.f}, s2[2] = {0.f, 0.f};
    auto stats = [&](int cc, int j) {
      const u32x4 v = ldsr<u32x4>(LX + cc * 8192 + j * 1024 + lw + lane * 16);
#pragma unroll
      for (int i = 0; i < 4; ++i) { const float lo = bflo(v[i]), hi = bfhi(v[i]); s1[j] += lo; s2[j] = __builtin_fmaf(lo, lo, s2[j]); s1[j] += hi; s2[j] = __builtin_fmaf(hi, hi, s2[j]); }
    };
#pragma unroll
    for (int cc = 0; cc < 8; ++cc) {
      if (cc == 6) VMBAR(4); else VMBAR(6);
      if (cc == 0) STAMP(1);
      if (cc < 6) {
        DMAW(2 * cc + 4, 1, cc + 2);
        if (cc == 0) { stats(0, 0); stats(0, 1); }
        stats(cc + 1, 0);
        DMAW(2 * cc + 5, 2, cc + 2);
        stats(cc + 1, 1);
        if (cc < 5) DMAX(cc + 3);
      } else if (cc == 6) {
        DMAW(16, 0, 0);
        stats(7, 0); stats(7, 1);
        DMAW(17, 0, 1);
#pragma unroll
        for (int j = 0; j < 2; ++j) {
          const float mu = sum8f(s1[j]) * (1.f / DM);
          const float var = __builtin_fmaxf(__builtin_fmaf(-mu, mu, sum8f(s2[j]) * (1.f / DM)), 0.f);
          const float rs = __builtin_amdgcn_rsqf(var + 1e-5f);
          if (s8 == 0) { ldsw<float>(L_STA + (16 * hd + 8 * j + rr) * 4, rs); ldsw<float>(L_STB + (16 * hd + 8 * j + rr) * 4, -mu * rs); }
        }
        DMAW(18, 0, 2);
      } else {
        STAMP(2);
        DMAW(19, 0, 3); DMAW(20, 0, 4); DMAW(21, 0, 5); DMAW(22, 0, 6); DMAW(23, 0, 7);
      }
    }
    f32x4 aq[4] = {}, cq = {};
#pragma unroll
    for (int cc = 0; cc < 8; ++cc) {
      if (cc == 0) VMBAR(8);
      if (cc == 4) VMBAR(0);
#pragma unroll
      for (int ks = 0; ks < 2; ++ks) {
        const u32x4 wf = trfrag(wb + ((16 + cc) % 10) * 8192 + ks * 4096, 512);
#pragma unroll
        for (int st = 0; st < 4; ++st) aq[st] = mfma16(wf, ldsr<u32x4>((ks ? xb1 : xb0) + cc * 8192 + st * 2048), aq[st]);
        cq = mfma16(wf, ones4, cq);
      }
    }
    tag = __builtin_amdgcn_readfirstlane(ldsr<u32>(L_EPO));
    constexpr float SC = 0.25f * 1.4426950408889634f;
    u32 q2[4][2];
#pragma unroll
    for (int st = 0; st < 4; ++st) {
      const float a = ldsr<float>(L_STA + (16 * st + c) * 4) * SC, bb = ldsr<float>(L_STB + (16 * st + c) * 4) * SC;
      q2[st][0] = pk(__builtin_fmaf(a, aq[st][0], bb * cq[0]), __builtin_fmaf(a, aq[st][1], bb * cq[1]));
      q2[st][1] = pk(__builtin_fmaf(a, aq[st][2], bb * cq[2]), __builtin_fmaf(a, aq[st][3], bb * cq[3]));
    }
    swap16(q2[0][0], q2[1][0]); swap16(q2[0][1], q2[1][1]); swap16(q2[2][0], q2[3][0]); swap16(q2[2][1], q2[3][1]);
    my1 = (u32x4){q2[0][0], q2[0][1], q2[1][0], q2[1][1]};
    my0 = (u32x4){q2[2][0], q2[2][1], q2[3][0], q2[3][1]};
    ldsw<u32x4>(L_QX + hd * 2048 + lane * 16, my1);
    ldsw<u32x4>(L_QX + hd * 2048 + 1024 + lane * 16, my0);
    STAMP(3);
  } else {
    if (wave == 0) { const srd_t srdR = mksrd(wr, DM * 2); const u32 vo = lane * 16; DMA(srdR, vo, zero, L_WR); }
    u32 ev;
    asm volatile("buffer_load_dword %0, %1, %2, %3 offen sc1\n\ts_waitcnt vmcnt(0)" : "=&v"(ev) : "v"(zero), "s"(srdS), "s"(zero) : "memory");
    ldsw<u32>(L_EPO, ev);
    tag = __builtin_amdgcn_readfirstlane(ev);
    f32x4 ak[4] = {}, av[4] = {}, ck = {}, cv = {};
#pragma unroll
    for (int cc = 0; cc < 8; ++cc) {
      BAR();
      if (cc == 0) STAMP(1);
      if (cc == 7) STAMP(2);
#pragma unroll
      for (int ks = 0; ks < 2; ++ks) {
        const u32x4 wk = trfrag(wb + ((2 * cc) % 10) * 8192 + ks * 4096, 512), wv = trfrag(wb + ((2 * cc + 1) % 10) * 8192 + ks * 4096, 512);
#pragma unroll
        for (int st = 0; st < 4; ++st) {
          const u32x4 xf = ldsr<u32x4>((ks ? xb1 : xb0) + cc * 8192 + st * 2048);
          ak[st] = mfma16(wk, xf, ak[st]);
          av[st] = mfma16(xf, wv, av[st]);
        }
        ck = mfma16(wk, ones4, ck);
        cv = mfma16(ones4, wv, cv);
      }
    }
    const u32 tg = ((tag + 1u) * 0x9E3779B1u) | 1u;
    const u32 vo16 = lane * 16, vo4 = (lane >> 1) * 4;
#pragma unroll
    for (int t = 0; t < 2; ++t) {
      u32 k2[2][2], v2[2][2];
#pragma unroll
      for (int u = 0; u < 2; ++u) {
        const int st = 2 * t + u;
        const float a = ldsr<float>(L_STA + (16 * st + c) * 4), bb = ldsr<float>(L_STB + (16 * st + c) * 4);
        k2[u][0] = pk(__builtin_fmaf(a, ak[st][0], bb * ck[0]), __builtin_fmaf(a, ak[st][1], bb * ck[1]));
        k2[u][1] = pk(__builtin_fmaf(a, ak[st][2], bb * ck[2]), __builtin_fmaf(a, ak[st][3], bb * ck[3]));
        const f32x4 a4 = ldsr<f32x4>(L_STA + (16 * st + 4 * g) * 4), b4 = ldsr<f32x4>(L_STB + (16 * st + 4 * g) * 4);
        v2[u][0] = pk(__builtin_fmaf(a4[0], av[st][0], b4[0] * cv[0]), __builtin_fmaf(a4[1], av[st][1], b4[1] * cv[0]));
        v2[u][1] = pk(__builtin_fmaf(a4[2], av[st][2], b4[2] * cv[0]), __builtin_fmaf(a4[3], av[st][3], b4[3] * cv[0]));
      }
      swap16(k2[0][0], k2[1][0]); swap16(k2[0][1], k2[1][1]);
      swap16(v2[0][0], v2[1][0]); swap16(v2[0][1], v2[1][1]);
      swap32(v2[0][0], v2[1][0]); swap32(v2[0][1], v2[1][1]);
      const u32x4 Kf = {k2[0][0], k2[0][1], k2[1][0], k2[1][1]}, Vf = {v2[0][0], v2[0][1], v2[1][0], v2[1][1]};
      const u32 so = WS_KV + ((tb * 32 + head) * 2 + t) * KV_UNIT;
      ST4(Kf, srdS, vo16, so, 0);
      ST4(Vf, srdS, vo16, so, 1024);
      u32 ch = sum4(Kf) + sum4(Vf);
      ch += dppu<DPP_X1>(ch) + tg;
      ST1(ch, srdS, vo4, so, 2048);
      if (t == 0) { my0 = Kf; my1 = Vf; BAR(); }
      else { ldsw<u32x4>(L_KVX + hd * 2048 + lane * 16, Kf); ldsw<u32x4>(L_KVX + hd * 2048 + 1024 + lane * 16, Vf); }
    }
    STAMP(3);
    BAR();
    asm volatile("s_waitcnt vmcnt(0)" ::: "memory");
  }
  STAMP(4);
  const u32 epoch = tag;
  tag = ((tag + 1u) * 0x9E3779B1u) | 1u;

  const u32 vo16 = lane * 16, vo4 = (lane >> 1) * 4;
  u32x4 Kn[4], Vn[4]; u32 Cn[4];
  srd_t srdN[4]; u32 soN[4];
#pragma unroll
  for (int n = 0; n < 4; ++n) { srdN[n] = mksrd(ws, tb > (u32)n ? WS_END : 0u); soN[n] = tb > (u32)n ? WS_KV + (((tb - 1 - n) * 32 + head) * 2 + p) * KV_UNIT : 0u; }
#define LDN(n) do { LD4(Kn[n], srdN[n], vo16, soN[n], 0); LD4(Vn[n], srdN[n], vo16, soN[n], 1024); LD1(Cn[n], srdN[n], vo4, soN[n], 2048); } while (0)
#define CHKN(n) do { for (u32 sp_ = 0; tb > (u32)(n) && sp_ < SPIN_CAP; ++sp_) { u32 ch_ = sum4(Kn[n]) + sum4(Vn[n]); ch_ += dppu<DPP_X1>(ch_) + tag; if (__all(ch_ == Cn[n])) break; \
    __builtin_amdgcn_s_sleep(2); \
    asm volatile("buffer_load_dwordx4 %0, %3, %5, %6 offen sc1\n\tbuffer_load_dwordx4 %1, %3, %5, %6 offen offset:1024 sc1\n\tbuffer_load_dword %2, %4, %5, %6 offen offset:2048 sc1\n\ts_waitcnt vmcnt(0)" \
                 : "+v"(Kn[n]), "+v"(Vn[n]), "+v"(Cn[n]) : "v"(vo16), "v"(vo4), "s"(srdN[n]), "s"(soN[n]) : "memory"); } } while (0)
  LDN(0); LDN(1);
  const u32 rho = hd, kap = p;
  float res[4][2];
  {
    const u32 base = LX + ((hq - r0) & 7) * 8192 + (16 * rho + 4 * g + 2 * kap) * 128 + (c & 7) * 2;
#pragma unroll
    for (int t = 0; t < 4; ++t) {
      const u32 a = base + (((2 * t + (c >> 3)) ^ (2 * g + kap)) * 16);
      res[t][0] = u2f((u32)ldsr<u16>(a) << 16); res[t][1] = u2f((u32)ldsr<u16>(a + 128) << 16);
    }
  }
  BAR();
  u32x4 Qown, Qoth, Kown, Rown;
  {
    const u32 a = (p ? L_KVX : L_QX) + hd * 2048 + lane * 16;
    const u32x4 r0v = ldsr<u32x4>(a), r1v = ldsr<u32x4>(a + 1024);
    if (p) { Qown = my0; Qoth = my1; Kown = r0v; Rown = r1v; } else { Qown = r0v; Qoth = r1v; Kown = my0; Rown = my1; }
  }
  const srd_t srdO = mksrd((const char*)wo + hq * 128, DM * DM * 2 - hq * 128);
  const u32 vwo = (8 * wave + rr) * 1024 + ((s8 ^ (rr & 6)) * 16);
#define DMAO(i) do { const u32 n_ = ((i) + tb) & 7; DMA(srdO, vwo, n_ * 65536, LX + n_ * 8192 + wave * 1024); } while (0)
  f32x16 aown = {}, aoth = {};
  u32x4 A0, A1;
  WAIT3(3, Kn[0], Vn[0], Cn[0]); CHKN(0);
  STAMP(5);
  LDN(2); DMAO(0); DMAO(1);
  vexpand(Vn[0], tb > 0 ? ONES : 0u, A0, A1);
  aoth = att_tile(Kn[0], Qoth, A0, A1, aoth); aown = att_tile(Kn[0], Qown, A0, A1, aown);
  WAIT3(5, Kn[1], Vn[1], Cn[1]); CHKN(1);
  LDN(3); DMAO(2); DMAO(3);
  vexpand(Vn[1], tb > 1 ? ONES : 0u, A0, A1);
  aoth = att_tile(Kn[1], Qoth, A0, A1, aoth); aown = att_tile(Kn[1], Qown, A0, A1, aown);
  WAIT3(7, Kn[2], Vn[2], Cn[2]); CHKN(2);
  DMAO(4); DMAO(5); DMAO(6); DMAO(7);
  vexpand(Vn[2], tb > 2 ? ONES : 0u, A0, A1);
  aoth = att_tile(Kn[2], Qoth, A0, A1, aoth); aown = att_tile(Kn[2], Qown, A0, A1, aown);
  WAIT3(6, Kn[3], Vn[3], Cn[3]); CHKN(3);
  STAMP(6);
  u32x4 F0, F1;
  vexpand(Vn[3], tb > 3 ? ONES : 0u, F0, F1);
  vexpand(Rown, ONES, A0, A1);
  aoth = att_tile(p ? Kn[3] : Kown, Qoth, p ? F0 : A0, p ? F1 : A1, aoth);
  {
    const u32 a = L_OX + wave * 2304;
    ldsw<f32x4>(a + lane * 16, (f32x4){aoth[0], aoth[1], aoth[2], aoth[3]});
    ldsw<f32x4>(a + 1024 + lane * 16, (f32x4){aoth[4], aoth[5], aoth[6], aoth[7]});
    ldsw<float>(a + 2048 + lane * 4, aoth[8]);
  }
  {
    const f32x16 z = {};
    const f32x16 sd = mfma32(Kown, Qown, z), sf = mfma32(Kn[3], Qown, z);
    const int qrel = (int)(lane & 31) - 4 * (int)(lane >> 5);
    u32x4 D[2], Fv[2];
#pragma unroll
    for (int i = 0; i < 8; ++i) {
      const int row = ((2 * i) & 3) + 8 * (i >> 1);
      const bool d0 = row <= qrel, d1 = row + 1 <= qrel;
      const u32 pp = pk(__builtin_amdgcn_exp2f(d0 ? sd[2 * i] : sf[2 * i]), __builtin_amdgcn_exp2f(d1 ? sd[2 * i + 1] : sf[2 * i + 1]));
      const u32 m = (d0 ? 0xffffu : 0u) | (d1 ? 0xffff0000u : 0u);
      D[i >> 2][i & 3] = pp & m; Fv[i >> 2][i & 3] = pp & ~m;
    }
    aown = mfma32(A0, D[0], aown); aown = mfma32(A1, D[1], aown);
    aown = mfma32(F0, Fv[0], aown); aown = mfma32(F1, Fv[1], aown);
  }
  VMBAR(0);
  STAMP(7);
  float o[8], l;
  {
    const u32 a = L_OX + (wave ^ 4) * 2304;
    const f32x4 u0 = ldsr<f32x4>(a + lane * 16), u1 = ldsr<f32x4>(a + 1024 + lane * 16);
    l = aown[8] + ldsr<float>(a + 2048 + lane * 4);
#pragma unroll
    for (int i = 0; i < 4; ++i) { o[i] = aown[i] + u0[i]; o[4 + i] = aown[4 + i] + u1[i]; }
  }
  if (FORCE_SLOW || __builtin_expect(!__all(l >= 0x1p-64f && l <= 0x1p64f), 0)) {
    const int qt = 2 * tb + p, q = lane & 31, hh = lane >> 5;
    const f32x16 z = {};
    float m = -__builtin_inff();
    f32x16 acc = {};
    for (int pass = 0; pass < 2; ++pass) {
      for (int kt = qt >= 8 ? qt - 8 : 0; kt <= qt; ++kt) {
        u32x4 K, R;
        const u32 so = WS_KV + (((kt >> 1) * 32 + head) * 2 + (kt & 1)) * KV_UNIT;
        asm volatile("buffer_load_dwordx4 %0, %2, %3, %4 offen sc1\n\tbuffer_load_dwordx4 %1, %2, %3, %4 offen offset:1024 sc1\n\ts_waitcnt vmcnt(0)"
                     : "=&v"(K), "=&v"(R) : "v"(vo16), "s"(srdS), "s"(so) : "memory");
        f32x16 s = mfma32(K, Qown, z);
#pragma unroll
        for (int i = 0; i < 16; ++i) {
          const int dist = (qt - kt) * 32 + q - ((i & 3) + 8 * (i >> 2) + 4 * hh);
          s[i] = (dist >= 0 && dist < 256) ? s[i] : -__builtin_inff();
        }
        if (pass == 0) {
#pragma unroll
          for (int i = 0; i < 16; ++i) m = __builtin_fmaxf(m, s[i]);
        } else {
          u32x4 P0, P1, B0, B1;
#pragma unroll
          for (int i = 0; i < 4; ++i) {
            P0[i] = pk(__builtin_amdgcn_exp2f(s[2 * i] - m), __builtin_amdgcn_exp2f(s[2 * i + 1] - m));
            P1[i] = pk(__builtin_amdgcn_exp2f(s[8 + 2 * i] - m), __builtin_amdgcn_exp2f(s[9 + 2 * i] - m));
          }
          vexpand(R, ONES, B0, B1);
          acc = mfma32(B0, P0, acc); acc = mfma32(B1, P1, acc);
        }
      }
      u32 ma = f2u(m), mb = ma; swap32(ma, mb); m = __builtin_fmaxf(u2f(ma), u2f(mb));
    }
#pragma unroll
    for (int i = 0; i < 8; ++i) o[i] = acc[i];
    l = acc[8];
  }
  {
    const float rl = __builtin_amdgcn_rcpf(l);
    const u32x4 of = {pk(o[0] * rl, o[1] * rl), pk(o[2] * rl, o[3] * rl), pk(o[4] * rl, o[5] * rl), pk(o[6] * rl, o[7] * rl)};
    const u32 vo = (g & 1) * 16384 + (g >> 1) * 256 + c * 16;
    const u32 so = WS_O + ((tb * 4 + 2 * p) * 16 + (head >> 1)) * 1024 + (head & 1) * 512;
    ST4(of, srdS, vo, so, 0);
    const u32 ch = rowsum16(sum4(of));
    if (c == 0) ldsw<u32>(L_OCX + (hd >> 1) * 64 + ((2 * p + (g & 1)) * 4 + 2 * (hd & 1) + (g >> 1)) * 4, ch);
  }
  BAR();
  u32 soC0 = WS_OC + tb * 1024;
  asm volatile("" : "+s"(soC0));
  if (wave == 0 && lane < 16) {
    const u32 ch = ldsr<u32>(L_OCX + lane * 4) + ldsr<u32>(L_OCX + 64 + lane * 4) + tag;
    const u32 vo = lane * 4, so = soC0 + hq * 128;
    ST1(ch, srdS, vo, so, 0);
  }
  STAMP(8);
  u32x4 Bw[8][4];
  {
    const u32 base = LX + (256 * kap + 16 * (g >> 1) + 4 * (g & 1) + ((lane >> 2) & 3)) * 128 + (lane & 3) * 8;
#pragma unroll
    for (int t = 0; t < 4; ++t)
#pragma unroll
      for (int m = 0; m < 8; ++m) Bw[m][t] = trfrag(base + ((t ^ wsw) * 32) + m * 4096, 1024);
  }
  float w4[4], wsum;
  {
    const u32x4 v = ldsr<u32x4>(L_WR + lane * 16);
    float sw = (bflo(v.x) + bfhi(v.x)) + (bflo(v.y) + bfhi(v.y)) + (bflo(v.z) + bfhi(v.z)) + (bflo(v.w) + bfhi(v.w));
    sw = sum8f(sw); sw += dppf<DPP_R8>(sw);
    u32 a = f2u(sw), bb = a; swap16(a, bb); sw = u2f(a) + u2f(bb);
    a = f2u(sw); bb = a; swap32(a, bb); wsum = u2f(a) + u2f(bb);
#pragma unroll
    for (int t = 0; t < 4; ++t) w4[t] = u2f((u32)ldsr<u16>(L_WR + (64 * hq + 16 * t + c) * 2) << 16);
  }
  asm volatile("s_waitcnt vmcnt(0) lgkmcnt(0)" ::: "memory");
  STAMP(9);
  u32x4 Of[8]; u32 Oc[4];
  const u32 soO = WS_O + ((tb * 4 + rho) * 16 + 8 * kap) * 1024, soO2 = soO + 4096, soC = soC0 + 512 * kap, voC = rho * 16 + g * 4;
  LD4(Of[0], srdS, vo16, soO, 0);     LD4(Of[1], srdS, vo16, soO, 1024);  LD1(Oc[0], srdS, voC, soC, 0);
  LD4(Of[2], srdS, vo16, soO, 2048);  LD4(Of[3], srdS, vo16, soO, 3072);  LD1(Oc[1], srdS, voC, soC, 128);
  LD4(Of[4], srdS, vo16, soO2, 0);    LD4(Of[5], srdS, vo16, soO2, 1024); LD1(Oc[2], srdS, voC, soC, 256);
  LD4(Of[6], srdS, vo16, soO2, 2048); LD4(Of[7], srdS, vo16, soO2, 3072); LD1(Oc[3], srdS, voC, soC, 384);
  {
    const u32 has = tb < 28;
    const srd_t srdG = mksrd(wg, has ? DM * FF * 2 : 0), srdU = mksrd(wu, has ? DM * FF * 2 : 0), srdD = mksrd(wd, has ? DM * FF * 2 : 0);
    const u32 sx = (s8 ^ ((rr & 2) | ((wave & 1) << 2))) * 16;
    const u32 vg = rr * (FF * 2) + sx, vd = rr * 1024 + sx;
    const u32 sg = (64 * hq + 8 * wave) * (FF * 2) + tb * 128, sd = (64 * tb + 8 * wave) * 1024 + hq * 128;
    DMA(srdG, vg, sg, L_FW + wave * 1024); DMA(srdU, vg, sg, L_FW + 8192 + wave * 1024); DMA(srdD, vd, sd, L_FW + 16384 + wave * 1024);
  }
  f32x4 ah[4];
#pragma unroll
  for (int t = 0; t < 4; ++t) ah[t] = kap ? (f32x4){0.f, 0.f, res[t][0], res[t][1]} : (f32x4){res[t][0], res[t][1], 0.f, 0.f};
#define CHKO(j, SO, I0, I1, IC) do { for (u32 sp_ = 0; sp_ < SPIN_CAP; ++sp_) { const u32 ch_ = rowsum16(sum4(Of[2 * j]) + sum4(Of[2 * j + 1])) + tag; if (__all(ch_ == Oc[j])) break; \
    __builtin_amdgcn_s_sleep(1); \
    asm volatile("buffer_load_dwordx4 %0, %3, %5, %6 offen offset:" #I0 " sc1\n\tbuffer_load_dwordx4 %1, %3, %5, %6 offen offset:" #I1 " sc1\n\tbuffer_load_dword %2, %4, %5, %7 offen offset:" #IC " sc1\n\ts_waitcnt vmcnt(0)" \
                 : "+v"(Of[2 * j]), "+v"(Of[2 * j + 1]), "+v"(Oc[j]) : "v"(vo16), "v"(voC), "s"(srdS), "s"(SO), "s"(soC) : "memory"); } \
    _Pragma("unroll") for (int t = 0; t < 4; ++t) ah[t] = mfma16(Of[2 * j], Bw[2 * j][t], ah[t]); \
    _Pragma("unroll") for (int t = 0; t < 4; ++t) ah[t] = mfma16(Of[2 * j + 1], Bw[2 * j + 1][t], ah[t]); } while (0)
  WAIT3(12, Of[0], Of[1], Oc[0]); CHKO(0, soO, 0, 1024, 0);
  WAIT3(9, Of[2], Of[3], Oc[1]);  CHKO(1, soO, 2048, 3072, 128);
  WAIT3(6, Of[4], Of[5], Oc[2]);  CHKO(2, soO2, 0, 1024, 256);
  WAIT3(3, Of[6], Of[7], Oc[3]);  CHKO(3, soO2, 2048, 3072, 384);
  STAMP(10);
  float h[4][2];
  {
    const u32 a = L_PX + wave * 2048 + lane * 16;
    f32x4 s0, s1v;
#pragma unroll
    for (int t = 0; t < 4; ++t) { s0[t] = kap ? ah[t][0] : ah[t][2]; s1v[t] = kap ? ah[t][1] : ah[t][3]; h[t][0] = kap ? ah[t][2] : ah[t][0]; h[t][1] = kap ? ah[t][3] : ah[t][1]; }
    ldsw<f32x4>(a, s0); ldsw<f32x4>(a + 1024, s1v);
    BAR();
    const u32 a2 = L_PX + (wave ^ 4) * 2048 + lane * 16;
    const f32x4 u0 = ldsr<f32x4>(a2), u1 = ldsr<f32x4>(a2 + 1024);
#pragma unroll
    for (int t = 0; t < 4; ++t) { h[t][0] += u0[t]; h[t][1] += u1[t]; }
  }
  float mu, rs, rsc;
  {
    float a1[2], a2[2], a3[2];
#pragma unroll
    for (int e = 0; e < 2; ++e) {
      a1[e] = (h[0][e] + h[1][e]) + (h[2][e] + h[3][e]);
      a2[e] = __builtin_fmaf(h[0][e], h[0][e], h[1][e] * h[1][e]) + __builtin_fmaf(h[2][e], h[2][e], h[3][e] * h[3][e]);
      a3[e] = __builtin_fmaf(h[0][e], w4[0], h[1][e] * w4[1]) + __builtin_fmaf(h[2][e], w4[2], h[3][e] * w4[3]);
    }
    const bool up = c >= 8;
    const float t1 = sum8f((up ? a1[1] : a1[0]) + dppf<DPP_R8>(up ? a1[0] : a1[1]));
    const float t2 = sum8f((up ? a2[1] : a2[0]) + dppf<DPP_R8>(up ? a2[0] : a2[1]));
    const float t3 = sum8f((up ? a3[1] : a3[0]) + dppf<DPP_R8>(up ? a3[0] : a3[1]));
    const u32x4 msg = {f2u(t1), f2u(t2), f2u(t3), f2u(t1) + f2u(t2) + f2u(t3) + tag};
    const u32 soS = WS_ST + (tb * 8 + wave) * 1024, vs = hq * 128 + rr * 16, vl = s8 * 128 + rr * 16;
    if (s8 == 0) ST4(msg, srdS, vs, soS, 0);
    STAMP(11);
    u32x4 v;
    for (u32 sp = 0; sp < SPIN_CAP; ++sp) {
      __builtin_amdgcn_s_sleep(2);
      asm volatile("buffer_load_dwordx4 %0, %1, %2, %3 offen sc1\n\ts_waitcnt vmcnt(0)" : "=&v"(v) : "v"(vl), "s"(srdS), "s"(soS) : "memory");
      if (__all(v.x + v.y + v.z + tag == v.w)) break;
    }
    STAMP(12);
    mu = sum8f(u2f(v.x)) * (1.f / DM);
    const float var = __builtin_fmaxf(__builtin_fmaf(-mu, mu, sum8f(u2f(v.y)) * (1.f / DM)), 0.f);
    rs = __builtin_amdgcn_rsqf(var + 1e-5f);
    rsc = rs * __builtin_fmaf(-mu, wsum, sum8f(u2f(v.z)));
  }
  {
    const u32 row = 16 * rho + 4 * g + 2 * kap;
    const u32 rb = f2u(rsc);
    const u32 key = ((rb ^ (((int)rb >> 31) | 0x80000000u)) & ~63u) | (63u - (row + (c >> 3)));
    if (s8 == 0) ldsw<u32>(L_KEY + (row + (c >> 3)) * 4, key);
    const bool up = c >= 8;
    const float muo = dppf<DPP_R8>(mu), rso = dppf<DPP_R8>(rs);
#pragma unroll
    for (int e = 0; e < 2; ++e) {
      const float m_ = (up == (e == 1)) ? mu : muo, r_ = (up == (e == 1)) ? rs : rso;
#pragma unroll
      for (int t = 0; t < 4; ++t) {
        const float ln = (h[t][e] - m_) * r_;
        const u32 pr = pk(ln, h[t][e] + ln);
        ldsw<u16>(L_LN + (row + e) * 128 + (16 * t + c) * 2, (u16)pr);
        ldsw<u16>(L_OUT + (row + e) * 128 + (16 * t + c) * 2, (u16)(pr >> 16));
      }
    }
  }
  BAR();
  u32 key = ldsr<u32>(L_KEY + lane * 4);
  key = max(key, dppu<DPP_X1>(key)); key = max(key, dppu<DPP_X2>(key)); key = max(key, dppu<DPP_HM>(key)); key = max(key, dppu<DPP_R8>(key));
  { u32 a = key, bb = key; swap16(a, bb); key = max(a, bb); a = key; bb = key; swap32(a, bb); key = max(a, bb); }
  const u32 crow = 63u - (key & 63u);
  const srd_t srdOut = mksrd((char*)out + (size_t)tb * 65536 + hq * 128, 65536 - hq * 128);
  if (wave == 0 && lane < 16) {
    const u32x2 d = ldsr<u32x2>(L_LN + crow * 128 + lane * 8);
    const u32x4 msg = {d.x, d.y, key, d.x + d.y + key + tag};
    const u32 so = WS_CAND + (hq * 32 + tb) * 256;
    ST4(msg, srdS, vo16, so, 0);
  }
  STAMP(13);
  {
    const u32 row = 8 * wave + rr;
    const u32x4 v = ldsr<u32x4>(L_OUT + row * 128 + s8 * 16);
    if (row != crow) __builtin_amdgcn_raw_buffer_store_b128(v, srdOut, row * 1024 + s8 * 16, 0, 0);
  }
  if (wave == 0) {
    u32x4 Wg[2][4], Wu[2][4], Wd[2][4];
#pragma unroll
    for (int t = 0; t < 4; ++t)
#pragma unroll
      for (int ks = 0; ks < 2; ++ks) {
        const u32 a = L_FW + wtr + ((t ^ wsw) * 32) + ks * 4096;
        Wg[ks][t] = trfrag(a, 512); Wu[ks][t] = trfrag(a + 8192, 512); Wd[ks][t] = trfrag(a + 16384, 512);
      }
    u32x4 m[8];
    const u32 soA = WS_CAND + hq * 8192, soB = soA + 4096;
#define LD8(SA, SB) asm volatile("buffer_load_dwordx4 %0, %8, %9, %10 offen sc1\n\tbuffer_load_dwordx4 %1, %8, %9, %10 offen offset:1024 sc1\n\t" \
      "buffer_load_dwordx4 %2, %8, %9, %10 offen offset:2048 sc1\n\tbuffer_load_dwordx4 %3, %8, %9, %10 offen offset:3072 sc1\n\t" \
      "buffer_load_dwordx4 %4, %8, %9, %11 offen sc1\n\tbuffer_load_dwordx4 %5, %8, %9, %11 offen offset:1024 sc1\n\t" \
      "buffer_load_dwordx4 %6, %8, %9, %11 offen offset:2048 sc1\n\tbuffer_load_dwordx4 %7, %8, %9, %11 offen offset:3072 sc1\n\ts_waitcnt vmcnt(0)" \
      : "=&v"(m[0]), "=&v"(m[1]), "=&v"(m[2]), "=&v"(m[3]), "=&v"(m[4]), "=&v"(m[5]), "=&v"(m[6]), "=&v"(m[7]) : "v"(vo16), "s"(srdS), "s"(SA), "s"(SB) : "memory")
    for (u32 sp = 0; sp < SPIN_CAP; ++sp) {
      __builtin_amdgcn_s_sleep(2);
      LD8(soA, soB);
      bool ok = true;
#pragma unroll
      for (int i = 0; i < 8; ++i) ok &= m[i].x + m[i].y + m[i].z + tag == m[i].w;
      if (__all(ok)) break;
    }
    STAMP(14);
    u32 bk = 0, d0 = 0, d1 = 0;
#pragma unroll
    for (int i = 0; i < 8; ++i) {
      const u32 k_ = (m[i].z & ~63u) | (63u - (4 * i + g));
      const bool gt = k_ > bk;
      bk = gt ? k_ : bk; d0 = gt ? m[i].x : d0; d1 = gt ? m[i].y : d1;
    }
    u32 gk;
    { u32 a = bk, bb = bk; swap16(a, bb); gk = max(a, bb); a = gk; bb = gk; swap32(a, bb); gk = max(a, bb); }
    if (bk == gk) ldsw<u32x2>(L_VEC + c * 8, (u32x2){d0, d1});
    const u32 wtb = 63u - (__builtin_amdgcn_readfirstlane(gk) & 63u);
    const f32x4 z4 = {};
    if (tb < 28) {
      const u32x4 xa0 = ldsr<u32x4>(L_VEC + g * 16), xa1 = ldsr<u32x4>(L_VEC + 64 + g * 16);
      float gp = 0.f, upv = 0.f;
#pragma unroll
      for (int t = 0; t < 4; ++t) {
        const f32x4 dg = mfma16(xa1, Wg[1][t], mfma16(xa0, Wg[0][t], z4)), du = mfma16(xa1, Wu[1][t], mfma16(xa0, Wu[0][t], z4));
        gp = g == (u32)t ? dg[0] : gp; upv = g == (u32)t ? du[0] : upv;
      }
      const u32x4 msg = {f2u(gp), f2u(upv), 0u, f2u(gp) + f2u(upv) + tag};
      const u32 so = WS_F1 + (tb * 8 + hq) * 1024;
      ST4(msg, srdS, vo16, so, 0);
    }
    STAMP(15);
    const u32 cbase = ldsr<u32>(L_OUT + crow * 128 + (lane & 31) * 4);
    if (wtb != tb && lane < 32) __builtin_amdgcn_raw_buffer_store_b32(cbase, srdOut, crow * 1024 + lane * 4, 0, 0);
    if (tb < 28) {
      const u32 sA = WS_F1 + tb * 8192, sB = sA + 4096;
      for (u32 sp = 0; sp < SPIN_CAP; ++sp) {
        __builtin_amdgcn_s_sleep(2);
        LD8(sA, sB);
        bool ok = true;
#pragma unroll
        for (int i = 0; i < 8; ++i) ok &= m[i].x + m[i].y + m[i].z + tag == m[i].w;
        if (__all(ok)) break;
      }
      STAMP(16);
      const float gs = ((u2f(m[0].x) + u2f(m[1].x)) + (u2f(m[2].x) + u2f(m[3].x))) + ((u2f(m[4].x) + u2f(m[5].x)) + (u2f(m[6].x) + u2f(m[7].x)));
      const float us = ((u2f(m[0].y) + u2f(m[1].y)) + (u2f(m[2].y) + u2f(m[3].y))) + ((u2f(m[4].y) + u2f(m[5].y)) + (u2f(m[6].y) + u2f(m[7].y)));
      const float gu = gs * __builtin_amdgcn_rcpf(1.f + __builtin_amdgcn_exp2f(gs * -1.4426950408889634f)) * us;
      ldsw<u16>(L_VEC + lane * 2, (u16)pk(gu, gu));
      const u32x4 ga0 = ldsr<u32x4>(L_VEC + g * 16), ga1 = ldsr<u32x4>(L_VEC + 64 + g * 16);
      float y = 0.f;
#pragma unroll
      for (int t = 0; t < 4; ++t) { const f32x4 dy = mfma16(ga1, Wd[1][t], mfma16(ga0, Wd[0][t], z4)); y = g == (u32)t ? dy[0] : y; }
      const u32x2 msg = {f2u(y), f2u(y) + tag};
      const u32 vo8 = lane * 8, so = WS_F2 + (hq * 28 + tb) * 512;
      ST2(msg, srdS, vo8, so, 0);
    }
    STAMP(17);
    if (wtb == tb) {
      float y0, y1;
      const u32 sA = WS_F2 + hq * (28 * 512);
      for (u32 sp = 0; sp < SPIN_CAP; ++sp) {
        __builtin_amdgcn_s_sleep(2);
        bool ok = true; y0 = 0.f; y1 = 0.f;
        u32x4 n[6];
        const u32 sC = sA + 8192, sD = sA + 12288;
        LD4(n[0], srdS, vo16, sC, 0); LD4(n[1], srdS, vo16, sC, 1024); LD4(n[2], srdS, vo16, sC, 2048); LD4(n[3], srdS, vo16, sC, 3072);
        LD4(n[4], srdS, vo16, sD, 0); LD4(n[5], srdS, vo16, sD, 1024);
        const u32 sB = sA + 4096;
        LD8(sA, sB);
        asm volatile("s_waitcnt vmcnt(0)" : "+v"(n[0]), "+v"(n[1]), "+v"(n[2]), "+v"(n[3]), "+v"(n[4]), "+v"(n[5]));
#pragma unroll
        for (int i = 0; i < 14; ++i) { const u32x4 v = i < 8 ? m[i & 7] : n[(i - 8) % 6]; ok &= (v.x + tag == v.y) & (v.z + tag == v.w); y0 += u2f(v.x); y1 += u2f(v.z); }
        if (__all(ok)) break;
      }
      STAMP(18);
      { u32 a = f2u(y0), bb = a; swap32(a, bb); y0 = u2f(a) + u2f(bb); a = f2u(y1); bb = a; swap32(a, bb); y1 = u2f(a) + u2f(bb); }
      const u32 kb = (key & ~63u) | 32u;
      const float rsel = u2f(kb ^ ((kb >> 31) ? 0x80000000u : 0xffffffffu));
      const float sg = __builtin_amdgcn_rcpf(1.f + __builtin_amdgcn_exp2f(rsel * -1.4426950408889634f));
      const u32 res2 = pk(__builtin_fmaf(y0, sg, bflo(cbase)), __builtin_fmaf(y1, sg, bfhi(cbase)));
      if (lane < 32) __builtin_amdgcn_raw_buffer_store_b32(res2, srdOut, crow * 1024 + lane * 4, 0, 0);
      if (hq == 0 && lane == 0) { const u32 e1 = epoch + 1u; ST1(e1, srdS, zero, zero, 0); }
    }
  }
}

extern "C" void kernel_launch(void* const* d_in, const int* in_sizes, int n_in, void* d_out, int out_size, void* d_ws, size_t ws_size,
                              hipStream_t stream) {
  fused_block<<<256, 512, 0, stream>>>((const u16*)d_in[0], (const u16*)d_in[1], (const u16*)d_in[2], (const u16*)d_in[4], (const u16*)d_in[5],
                                       (const u16*)d_in[6], (const u16*)d_in[7], (u16*)d_out, (char*)d_ws);
}
```

```cpp
#define PROBE 103
#include <hip/hip_runtime.h>
#include <stdint.h>

typedef unsigned u32;
typedef unsigned short u16;
typedef u32 u32x4 __attribute__((ext_vector_type(4)));
typedef u32 u32x2 __attribute__((ext_vector_type(2)));
typedef float f32x2 __attribute__((ext_vector_type(2)));
typedef float f32x4 __attribute__((ext_vector_type(4)));
typedef float f32x16 __attribute__((ext_vector_type(16)));
typedef short s16x4 __attribute__((ext_vector_type(4)));
typedef __bf16 bf16x2 __attribute__((ext_vector_type(2)));
typedef __bf16 bf16x8 __attribute__((ext_vector_type(8)));
typedef __amdgpu_buffer_rsrc_t srd_t;
#define LAS __attribute__((address_space(3)))
#define DI __device__ __forceinline__

#define STAMPS 0
#define FORCE_SLOW 0

constexpr int SEQ = 2048, DM = 512, FF = 1792;
constexpr u32 WS_KV = 4096, KV_UNIT = 2176;
constexpr u32 WS_O = 5u << 20;
constexpr u32 WS_OC = 7u << 20;
constexpr u32 WS_ST = WS_OC + 65536;
constexpr u32 WS_CAND = WS_ST + 262144;
constexpr u32 WS_F1 = WS_CAND + 65536;
constexpr u32 WS_F2 = WS_F1 + 262144;
constexpr u32 WS_END = WS_F2 + 131072;
constexpr u32 WS_DBG = 16u << 20;
constexpr u32 LX = 0;
constexpr u32 LR = 65536;
constexpr u32 LM = LR + 10 * 8192;
constexpr u32 L_KVX = LM;
constexpr u32 L_STA = LM + 8192, L_STB = L_STA + 256;
constexpr u32 L_WR = L_STB + 256;
constexpr u32 L_OCX = L_WR + 1024;
constexpr u32 L_KEY = L_OCX + 128;
constexpr u32 L_VEC = L_KEY + 256;
constexpr u32 L_EPO = L_VEC + 128;
constexpr u32 L_QX = LR + 4 * 8192;
constexpr u32 L_OX = LR;
constexpr u32 L_PX = LR + 5 * 8192;
constexpr u32 L_FW = LR + 7 * 8192;
constexpr u32 L_LN = LR;
constexpr u32 L_OUT = LR + 8192;
constexpr u32 LDS_BYTES = 163840;
constexpr u32 SPIN_CAP = 1u << 16;
constexpr u32 ONES = 0x3F803F80u;

DI u32 f2u(float f) { return __builtin_bit_cast(u32, f); }
DI float u2f(u32 u) { return __builtin_bit_cast(float, u); }
DI u32 pk(float lo, float hi) { f32x2 v = {lo, hi}; return __builtin_bit_cast(u32, __builtin_convertvector(v, bf16x2)); }
DI float bflo(u32 d) { return u2f(d << 16); }
DI float bfhi(u32 d) { return u2f(d & 0xffff0000u); }
DI f32x4 mfma16(u32x4 a, u32x4 b, f32x4 c) { return __builtin_amdgcn_mfma_f32_16x16x32_bf16(__builtin_bit_cast(bf16x8, a), __builtin_bit_cast(bf16x8, b), c, 0, 0, 0); }
DI f32x16 mfma32(u32x4 a, u32x4 b, f32x16 c) { return __builtin_amdgcn_mfma_f32_32x32x16_bf16(__builtin_bit_cast(bf16x8, a), __builtin_bit_cast(bf16x8, b), c, 0, 0, 0); }
template <class T> DI T ldsr(u32 a) { return *(const LAS T*)(size_t)a; }
template <class T> DI void ldsw(u32 a, T v) { *(LAS T*)(size_t)a = v; }
DI u32x2 ldstr(u32 a) { return __builtin_bit_cast(u32x2, __builtin_amdgcn_ds_read_tr16_b64_v4i16((LAS s16x4*)(size_t)a)); }
DI u32x4 trfrag(u32 a, u32 step) { u32x2 lo = ldstr(a), hi = ldstr(a + step); return (u32x4){lo.x, lo.y, hi.x, hi.y}; }
template <int CTRL> DI u32 dppu(u32 v) { return __builtin_amdgcn_mov_dpp(v, CTRL, 0xf, 0xf, true); }
template <int CTRL> DI float dppf(float v) { return u2f(dppu<CTRL>(f2u(v))); }
constexpr int DPP_X1 = 0xB1, DPP_X2 = 0x4E, DPP_HM = 0x141, DPP_R8 = 0x128;
DI void swap16(u32& a, u32& b) { auto r = __builtin_amdgcn_permlane16_swap(a, b, false, false); a = r[0]; b = r[1]; }
DI void swap32(u32& a, u32& b) { auto r = __builtin_amdgcn_permlane32_swap(a, b, false, false); a = r[0]; b = r[1]; }
DI u32 sum4(u32x4 v) { return v.x + v.y + v.z + v.w; }
DI u32 rowsum16(u32 v) { v += dppu<DPP_X1>(v); v += dppu<DPP_X2>(v); v += dppu<DPP_HM>(v); v += dppu<DPP_R8>(v); return v; }
DI float sum8f(float v) { v += dppf<DPP_X1>(v); v += dppf<DPP_X2>(v); v += dppf<DPP_HM>(v); return v; }
DI srd_t mksrd(const void* p, u32 bytes) { return __builtin_amdgcn_make_buffer_rsrc((void*)p, (short)0, (int)bytes, 0x00020000); }

#define BAR() asm volatile("s_waitcnt lgkmcnt(0)\n\ts_barrier" ::: "memory")
#define VMBAR(N) asm volatile("s_waitcnt vmcnt(" #N ") lgkmcnt(0)\n\ts_barrier" ::: "memory")
#define DMA(srd, voff, soff, ldst) asm volatile("s_mov_b32 m0, %0\n\ts_nop 0\n\tbuffer_load_dwordx4 %1, %2, %3 offen lds" ::"s"(ldst), "v"(voff), "s"(srd), "s"(soff) : "memory")
#define LD4(dst, srd, voff, soff, imm) asm volatile("buffer_load_dwordx4 %0, %1, %2, %3 offen offset:" #imm " sc1" : "=v"(dst) : "v"(voff), "s"(srd), "s"(soff) : "memory")
#define LD1(dst, srd, voff, soff, imm) asm volatile("buffer_load_dword %0, %1, %2, %3 offen offset:" #imm " sc1" : "=v"(dst) : "v"(voff), "s"(srd), "s"(soff) : "memory")
#define ST4(src, srd, voff, soff, imm) asm volatile("buffer_store_dwordx4 %0, %1, %2, %3 offen offset:" #imm " sc1\n\ts_nop 1" ::"v"(src), "v"(voff), "s"(srd), "s"(soff) : "memory")
#define ST2(src, srd, voff, soff, imm) asm volatile("buffer_store_dwordx2 %0, %1, %2, %3 offen offset:" #imm " sc1" ::"v"(src), "v"(voff), "s"(srd), "s"(soff) : "memory")
#define ST1(src, srd, voff, soff, imm) asm volatile("buffer_store_dword %0, %1, %2, %3 offen offset:" #imm " sc1" ::"v"(src), "v"(voff), "s"(srd), "s"(soff) : "memory")
#define WAIT3(N, a, b, c) asm volatile("s_waitcnt vmcnt(" #N ")" : "+v"(a), "+v"(b), "+v"(c)::"memory")

#undef STAMPS
#define STAMPS 1
#define STAMP(i) do { unsigned long long t_; asm volatile("s_memrealtime %0\n\ts_waitcnt lgkmcnt(0)" : "=s"(t_)::"memory"); stamp[i] = (u32)t_; } while (0)

DI f32x16 att_tile(u32x4 K, u32x4 Q, u32x4 A0, u32x4 A1, f32x16 acc) {
  const f32x16 z = {};
  f32x16 s = mfma32(K, Q, z);
  u32x4 P0, P1;
#pragma unroll
  for (int i = 0; i < 4; ++i) {
    P0[i] = pk(__builtin_amdgcn_exp2f(s[2 * i]), __builtin_amdgcn_exp2f(s[2 * i + 1]));
    P1[i] = pk(__builtin_amdgcn_exp2f(s[8 + 2 * i]), __builtin_amdgcn_exp2f(s[9 + 2 * i]));
  }
  acc = mfma32(A0, P0, acc);
  return mfma32(A1, P1, acc);
}
DI void vexpand(u32x4 R, u32 ones, u32x4& A0, u32x4& A1) {
#pragma unroll
  for (int i = 0; i < 4; ++i) { u32 a = R[i], b = ones; swap16(a, b); A0[i] = a; A1[i] = b; }
}

__global__ __launch_bounds__(512, 2) void fused_block(const u16* __restrict__ x, const u16* __restrict__ wqkv, const u16* __restrict__ wo,
                                                      const u16* __restrict__ wr, const u16* __restrict__ wg, const u16* __restrict__ wu,
                                                      const u16* __restrict__ wd, u16* out, char* ws) {
  __shared__ __attribute__((aligned(16))) char lds_[LDS_BYTES];
  asm volatile("" ::"s"((u32)(size_t)lds_));
  u32 stamp[20] = {};
  STAMP(0);
  const unsigned long long cyc0 = __builtin_readcyclecounter();
  const u32 lane = threadIdx.x & 63;
  const u32 wave = __builtin_amdgcn_readfirstlane(threadIdx.x >> 6);
  const u32 b = blockIdx.x, xg = b & 7, bi = b >> 3;
  const u32 tb = (xg >> 1) * 8 + (bi & 7), hq = (xg & 1) * 4 + (bi >> 3);
  const u32 hd = wave & 3, p = wave >> 2;
  const u32 c = lane & 15, g = lane >> 4, rr = lane >> 3, s8 = lane & 7;
  const u32 r0 = (tb + 2 * hq) & 7;
  const srd_t srdS = mksrd(ws, WS_END);
  const u32 zero = 0;
  u32x4 my0, my1;
  u32 tag;
  const u32 xb0 = LX + c * 128 + ((g ^ (c >> 1)) * 16), xb1 = xb0 ^ 64;
  const u32 wsw = ((lane >> 3) & 1) | ((g & 1) << 1);
  const u32 wtr = (8 * g + ((lane >> 2) & 3)) * 128 + (lane & 3) * 8;
  const u32 wb = LR + wtr + ((hd ^ wsw) * 32);
  const u32x4 ones4 = {ONES, ONES, ONES, ONES};
  const u32 head = hq * 4 + hd;

  if (p == 1) {
    const srd_t srdX = mksrd((const char*)x + (size_t)tb * 65536, 65536);
    const srd_t srdW = mksrd((const char*)wqkv + hq * 128, DM * 3 * DM * 2 - hq * 128);
    const u32 vx0 = (16 * hd + rr) * 1024 + ((s8 ^ (rr >> 1)) * 16), vx1 = (vx0 + 8192) ^ 64;
    const u32 vw0 = (16 * hd + rr) * 3072 + ((s8 ^ (rr & 2)) * 16), vw1 = (vw0 + 8 * 3072) ^ 64;
    const u32 lw = hd * 2048;
#define DMAX(cc) do { const u32 so_ = (((cc) + r0) & 7) * 128; DMA(srdX, vx0, so_, lw + (LX + (cc) * 8192)); DMA(srdX, vx1, so_, lw + (LX + (cc) * 8192 + 1024)); } while (0)
#define DMAW(n, part, cc) do { const u32 so_ = (((cc) + r0) & 7) * (64 * 3072) + (part) * 1024; DMA(srdW, vw0, so_, lw + (LR + ((n) % 10) * 8192)); DMA(srdW, vw1, so_, lw + (LR + ((n) % 10) * 8192 + 1024)); } while (0)
    DMAX(0); DMAW(0, 1, 0); DMAW(1, 2, 0); DMAX(1);
    DMAW(2, 1, 1); DMAW(3, 2, 1); DMAX(2);
    float s1[2] = {0.f, 0.f}, s2[2] = {0.f, 0.f};
    auto stats = [&](int cc, int j) {
      const u32x4 v = ldsr<u32x4>(LX + cc * 8192 + j * 1024 + lw + lane * 16);
#pragma unroll
      for (int i = 0; i < 4; ++i) { const float lo = bflo(v[i]), hi = bfhi(v[i]); s1[j] += lo; s2[j] = __builtin_fmaf(lo, lo, s2[j]); s1[j] += hi; s2[j] = __builtin_fmaf(hi, hi, s2[j]); }
    };
#pragma unroll
    for (int cc = 0; cc < 8; ++cc) {
      if (cc == 6) VMBAR(4); else VMBAR(6);
      if (cc == 0) STAMP(1);
      if (cc < 6) {
        DMAW(2 * cc + 4, 1, cc + 2);
        if (cc == 0) { stats(0, 0); stats(0, 1); }
        stats(cc + 1, 0);
        DMAW(2 * cc + 5, 2, cc + 2);
        stats(cc + 1, 1);
        if (cc < 5) DMAX(cc + 3);
      } else if (cc == 6) {
        DMAW(16, 0, 0);
        stats(7, 0); stats(7, 1);
        DMAW(17, 0, 1);
#pragma unroll
        for (int j = 0; j < 2; ++j) {
          const float mu = sum8f(s1[j]) * (1.f / DM);
          const float var = __builtin_fmaxf(__builtin_fmaf(-mu, mu, sum8f(s2[j]) * (1.f / DM)), 0.f);
          const float rs = __builtin_amdgcn_rsqf(var + 1e-5f);
          if (s8 == 0) { ldsw<float>(L_STA + (16 * hd + 8 * j + rr) * 4, rs); ldsw<float>(L_STB + (16 * hd + 8 * j + rr) * 4, -mu * rs); }
        }
        DMAW(18, 0, 2);
      } else {
        STAMP(2);
        DMAW(19, 0, 3); DMAW(20, 0, 4); DMAW(21, 0, 5); DMAW(22, 0, 6); DMAW(23, 0, 7);
      }
    }
    f32x4 aq[4] = {}, cq = {};
#pragma unroll
    for (int cc = 0; cc < 8; ++cc) {
      if (cc == 0) VMBAR(8);
      if (cc == 4) VMBAR(0);
#pragma unroll
      for (int ks = 0; ks < 2; ++ks) {
        const u32x4 wf = trfrag(wb + ((16 + cc) % 10) * 8192 + ks * 4096, 512);
#pragma unroll
        for (int st = 0; st < 4; ++st) aq[st] = mfma16(wf, ldsr<u32x4>((ks ? xb1 : xb0) + cc * 8192 + st * 2048), aq[st]);
        cq = mfma16(wf, ones4, cq);
      }
    }
    tag = __builtin_amdgcn_readfirstlane(ldsr<u32>(L_EPO));
    constexpr float SC = 0.25f * 1.4426950408889634f;
    u32 q2[4][2];
#pragma unroll
    for (int st = 0; st < 4; ++st) {
      const float a = ldsr<float>(L_STA + (16 * st + c) * 4) * SC, bb = ldsr<float>(L_STB + (16 * st + c) * 4) * SC;
      q2[st][0] = pk(__builtin_fmaf(a, aq[st][0], bb * cq[0]), __builtin_fmaf(a, aq[st][1], bb * cq[1]));
      q2[st][1] = pk(__builtin_fmaf(a, aq[st][2], bb * cq[2]), __builtin_fmaf(a, aq[st][3], bb * cq[3]));
    }
    swap16(q2[0][0], q2[1][0]); swap16(q2[0][1], q2[1][1]); swap16(q2[2][0], q2[3][0]); swap16(q2[2][1], q2[3][1]);
    my1 = (u32x4){q2[0][0], q2[0][1], q2[1][0], q2[1][1]};
    my0 = (u32x4){q2[2][0], q2[2][1], q2[3][0], q2[3][1]};
    ldsw<u32x4>(L_QX + hd * 2048 + lane * 16, my1);
    ldsw<u32x4>(L_QX + hd * 2048 + 1024 + lane * 16, my0);
    STAMP(3);
  } else {
    if (wave == 0) { const srd_t srdR = mksrd(wr, DM * 2); const u32 vo = lane * 16; DMA(srdR, vo, zero, L_WR); }
    u32 ev;
    if (wave == 0 && lane == 0) __hip_atomic_store((__attribute__((address_space(1))) u32*)(ws + WS_DBG + b * 4), stamp[0], __ATOMIC_RELAXED, __HIP_MEMORY_SCOPE_AGENT);
    asm volatile("buffer_load_dword %0, %1, %2, %3 offen sc1\n\ts_waitcnt vmcnt(0)" : "=&v"(ev) : "v"(zero), "s"(srdS), "s"(zero) : "memory");
    ldsw<u32>(L_EPO, ev);
    tag = __builtin_amdgcn_readfirstlane(ev);
    f32x4 ak[4] = {}, av[4] = {}, ck = {}, cv = {};
#pragma unroll
    for (int cc = 0; cc < 8; ++cc) {
      BAR();
      if (cc == 0) STAMP(1);
      if (cc == 7) STAMP(2);
#pragma unroll
      for (int ks = 0; ks < 2; ++ks) {
        const u32x4 wk = trfrag(wb + ((2 * cc) % 10) * 8192 + ks * 4096, 512), wv = trfrag(wb + ((2 * cc + 1) % 10) * 8192 + ks * 4096, 512);
#pragma unroll
        for (int st = 0; st < 4; ++st) {
          const u32x4 xf = ldsr<u32x4>((ks ? xb1 : xb0) + cc * 8192 + st * 2048);
          ak[st] = mfma16(wk, xf, ak[st]);
          av[st] = mfma16(xf, wv, av[st]);
        }
        ck = mfma16(wk, ones4, ck);
        cv = mfma16(ones4, wv, cv);
      }
    }
    const u32 tg = ((tag + 1u) * 0x9E3779B1u) | 1u;
    const u32 vo16 = lane * 16, vo4 = (lane >> 1) * 4;
#pragma unroll
    for (int t = 0; t < 2; ++t) {
      u32 k2[2][2], v2[2][2];
#pragma unroll
      for (int u = 0; u < 2; ++u) {
        const int st = 2 * t + u;
        const float a = ldsr<float>(L_STA + (16 * st + c) * 4), bb = ldsr<float>(L_STB + (16 * st + c) * 4);
        k2[u][0] = pk(__builtin_fmaf(a, ak[st][0], bb * ck[0]), __builtin_fmaf(a, ak[st][1], bb * ck[1]));
        k2[u][1] = pk(__builtin_fmaf(a, ak[st][2], bb * ck[2]), __builtin_fmaf(a, ak[st][3], bb * ck[3]));
        const f32x4 a4 = ldsr<f32x4>(L_STA + (16 * st + 4 * g) * 4), b4 = ldsr<f32x4>(L_STB + (16 * st + 4 * g) * 4);
        v2[u][0] = pk(__builtin_fmaf(a4[0], av[st][0], b4[0] * cv[0]), __builtin_fmaf(a4[1], av[st][1], b4[1] * cv[0]));
        v2[u][1] = pk(__builtin_fmaf(a4[2], av[st][2], b4[2] * cv[0]), __builtin_fmaf(a4[3], av[st][3], b4[3] * cv[0]));
      }
      swap16(k2[0][0], k2[1][0]); swap16(k2[0][1], k2[1][1]);
      swap16(v2[0][0], v2[1][0]); swap16(v2[0][1], v2[1][1]);
      swap32(v2[0][0], v2[1][0]); swap32(v2[0][1], v2[1][1]);
      const u32x4 Kf = {k2[0][0], k2[0][1], k2[1][0], k2[1][1]}, Vf = {v2[0][0], v2[0][1], v2[1][0], v2[1][1]};
      const u32 so = WS_KV + ((tb * 32 + head) * 2 + t) * KV_UNIT;
      ST4(Kf, srdS, vo16, so, 0);
      ST4(Vf, srdS, vo16, so, 1024);
      u32 ch = sum4(Kf) + sum4(Vf);
      ch += dppu<DPP_X1>(ch) + tg;
      ST1(ch, srdS, vo4, so, 2048);
      if (t == 0) { my0 = Kf; my1 = Vf; BAR(); }
      else { ldsw<u32x4>(L_KVX + hd * 2048 + lane * 16, Kf); ldsw<u32x4>(L_KVX + hd * 2048 + 1024 + lane * 16, Vf); }
    }
    STAMP(3);
    BAR();
    asm volatile("s_waitcnt vmcnt(0)" ::: "memory");
  }
  STAMP(4);
  const u32 epoch = tag;
  tag = ((tag + 1u) * 0x9E3779B1u) | 1u;

  const u32 vo16 = lane * 16, vo4 = (lane >> 1) * 4;
  u32x4 Kn[4], Vn[4]; u32 Cn[4];
  srd_t srdN[4]; u32 soN[4];
#pragma unroll
  for (int n = 0; n < 4; ++n) { srdN[n] = mksrd(ws, tb > (u32)n ? WS_END : 0u); soN[n] = tb > (u32)n ? WS_KV + (((tb - 1 - n) * 32 + head) * 2 + p) * KV_UNIT : 0u; }
#define LDN(n) do { LD4(Kn[n], srdN[n], vo16, soN[n], 0); LD4(Vn[n], srdN[n], vo16, soN[n], 1024); LD1(Cn[n], srdN[n], vo4, soN[n], 2048); } while (0)
#define CHKN(n) do { for (u32 sp_ = 0; tb > (u32)(n) && sp_ < SPIN_CAP; ++sp_) { u32 ch_ = sum4(Kn[n]) + sum4(Vn[n]); ch_ += dppu<DPP_X1>(ch_) + tag; if (__all(ch_ == Cn[n])) break; \
    __builtin_amdgcn_s_sleep(2); \
    asm volatile("buffer_load_dwordx4 %0, %3, %5, %6 offen sc1\n\tbuffer_load_dwordx4 %1, %3, %5, %6 offen offset:1024 sc1\n\tbuffer_load_dword %2, %4, %5, %6 offen offset:2048 sc1\n\ts_waitcnt vmcnt(0)" \
                 : "+v"(Kn[n]), "+v"(Vn[n]), "+v"(Cn[n]) : "v"(vo16), "v"(vo4), "s"(srdN[n]), "s"(soN[n]) : "memory"); } } while (0)
  LDN(0); LDN(1);
  const u32 rho = hd, kap = p;
  float res[4][2];
  {
    const u32 base = LX + ((hq - r0) & 7) * 8192 + (16 * rho + 4 * g + 2 * kap) * 128 + (c & 7) * 2;
#pragma unroll
    for (int t = 0; t < 4; ++t) {
      const u32 a = base + (((2 * t + (c >> 3)) ^ (2 * g + kap)) * 16);
      res[t][0] = u2f((u32)ldsr<u16>(a) << 16); res[t][1] = u2f((u32)ldsr<u16>(a + 128) << 16);
    }
  }
  BAR();
  u32x4 Qown, Qoth, Kown, Rown;
  {
    const u32 a = (p ? L_KVX : L_QX) + hd * 2048 + lane * 16;
    const u32x4 r0v = ldsr<u32x4>(a), r1v = ldsr<u32x4>(a + 1024);
    if (p) { Qown = my0; Qoth = my1; Kown = r0v; Rown = r1v; } else { Qown = r0v; Qoth = r1v; Kown = my0; Rown = my1; }
  }
  const srd_t srdO = mksrd((const char*)wo + hq * 128, DM * DM * 2 - hq * 128);
  const u32 vwo = (8 * wave + rr) * 1024 + ((s8 ^ (rr & 6)) * 16);
#define DMAO(i) do { const u32 n_ = ((i) + tb) & 7; DMA(srdO, vwo, n_ * 65536, LX + n_ * 8192 + wave * 1024); } while (0)
  f32x16 aown = {}, aoth = {};
  u32x4 A0, A1;
  WAIT3(3, Kn[0], Vn[0], Cn[0]); CHKN(0);
  STAMP(5);
  LDN(2); DMAO(0); DMAO(1);
  vexpand(Vn[0], tb > 0 ? ONES : 0u, A0, A1);
  aoth = att_tile(Kn[0], Qoth, A0, A1, aoth); aown = att_tile(Kn[0], Qown, A0, A1, aown);
  WAIT3(5, Kn[1], Vn[1], Cn[1]); CHKN(1);
  LDN(3); DMAO(2); DMAO(3);
  vexpand(Vn[1], tb > 1 ? ONES : 0u, A0, A1);
  aoth = att_tile(Kn[1], Qoth, A0, A1, aoth); aown = att_tile(Kn[1], Qown, A0, A1, aown);
  WAIT3(7, Kn[2], Vn[2], Cn[2]); CHKN(2);
  DMAO(4); DMAO(5); DMAO(6); DMAO(7);
  vexpand(Vn[2], tb > 2 ? ONES : 0u, A0, A1);
  aoth = att_tile(Kn[2], Qoth, A0, A1, aoth); aown = att_tile(Kn[2], Qown, A0, A1, aown);
  WAIT3(6, Kn[3], Vn[3], Cn[3]); CHKN(3);
  STAMP(6);
  u32x4 F0, F1;
  vexpand(Vn[3], tb > 3 ? ONES : 0u, F0, F1);
  vexpand(Rown, ONES, A0, A1);
  aoth = att_tile(p ? Kn[3] : Kown, Qoth, p ? F0 : A0, p ? F1 : A1, aoth);
  {
    const u32 a = L_OX + wave * 2304;
    ldsw<f32x4>(a + lane * 16, (f32x4){aoth[0], aoth[1], aoth[2], aoth[3]});
    ldsw<f32x4>(a + 1024 + lane * 16, (f32x4){aoth[4], aoth[5], aoth[6], aoth[7]});
    ldsw<float>(a + 2048 + lane * 4, aoth[8]);
  }
  {
    const f32x16 z = {};
    const f32x16 sd = mfma32(Kown, Qown, z), sf = mfma32(Kn[3], Qown, z);
    const int qrel = (int)(lane & 31) - 4 * (int)(lane >> 5);
    u32x4 D[2], Fv[2];
#pragma unroll
    for (int i = 0; i < 8; ++i) {
      const int row = ((2 * i) & 3) + 8 * (i >> 1);
      const bool d0 = row <= qrel, d1 = row + 1 <= qrel;
      const u32 pp = pk(__builtin_amdgcn_exp2f(d0 ? sd[2 * i] : sf[2 * i]), __builtin_amdgcn_exp2f(d1 ? sd[2 * i + 1] : sf[2 * i + 1]));
      const u32 m = (d0 ? 0xffffu : 0u) | (d1 ? 0xffff0000u : 0u);
      D[i >> 2][i & 3] = pp & m; Fv[i >> 2][i & 3] = pp & ~m;
    }
    aown = mfma32(A0, D[0], aown); aown = mfma32(A1, D[1], aown);
    aown = mfma32(F0, Fv[0], aown); aown = mfma32(F1, Fv[1], aown);
  }
  VMBAR(0);
  STAMP(7);
  float o[8], l;
  {
    const u32 a = L_OX + (wave ^ 4) * 2304;
    const f32x4 u0 = ldsr<f32x4>(a + lane * 16), u1 = ldsr<f32x4>(a + 1024 + lane * 16);
    l = aown[8] + ldsr<float>(a + 2048 + lane * 4);
#pragma unroll
    for (int i = 0; i < 4; ++i) { o[i] = aown[i] + u0[i]; o[4 + i] = aown[4 + i] + u1[i]; }
  }
  if (FORCE_SLOW || __builtin_expect(!__all(l >= 0x1p-64f && l <= 0x1p64f), 0)) {
    const int qt = 2 * tb + p, q = lane & 31, hh = lane >> 5;
    const f32x16 z = {};
    float m = -__builtin_inff();
    f32x16 acc = {};
    for (int pass = 0; pass < 2; ++pass) {
      for (int kt = qt >= 8 ? qt - 8 : 0; kt <= qt; ++kt) {
        u32x4 K, R;
        const u32 so = WS_KV + (((kt >> 1) * 32 + head) * 2 + (kt & 1)) * KV_UNIT;
        asm volatile("buffer_load_dwordx4 %0, %2, %3, %4 offen sc1\n\tbuffer_load_dwordx4 %1, %2, %3, %4 offen offset:1024 sc1\n\ts_waitcnt vmcnt(0)"
                     : "=&v"(K), "=&v"(R) : "v"(vo16), "s"(srdS), "s"(so) : "memory");
        f32x16 s = mfma32(K, Qown, z);
#pragma unroll
        for (int i = 0; i < 16; ++i) {
          const int dist = (qt - kt) * 32 + q - ((i & 3) + 8 * (i >> 2) + 4 * hh);
          s[i] = (dist >= 0 && dist < 256) ? s[i] : -__builtin_inff();
        }
        if (pass == 0) {
#pragma unroll
          for (int i = 0; i < 16; ++i) m = __builtin_fmaxf(m, s[i]);
        } else {
          u32x4 P0, P1, B0, B1;
#pragma unroll
          for (int i = 0; i < 4; ++i) {
            P0[i] = pk(__builtin_amdgcn_exp2f(s[2 * i] - m), __builtin_amdgcn_exp2f(s[2 * i + 1] - m));
            P1[i] = pk(__builtin_amdgcn_exp2f(s[8 + 2 * i] - m), __builtin_amdgcn_exp2f(s[9 + 2 * i] - m));
          }
          vexpand(R, ONES, B0, B1);
          acc = mfma32(B0, P0, acc); acc = mfma32(B1, P1, acc);
        }
      }
      u32 ma = f2u(m), mb = ma; swap32(ma, mb); m = __builtin_fmaxf(u2f(ma), u2f(mb));
    }
#pragma unroll
    for (int i = 0; i < 8; ++i) o[i] = acc[i];
    l = acc[8];
  }
  {
    const float rl = __builtin_amdgcn_rcpf(l);
    const u32x4 of = {pk(o[0] * rl, o[1] * rl), pk(o[2] * rl, o[3] * rl), pk(o[4] * rl, o[5] * rl), pk(o[6] * rl, o[7] * rl)};
    const u32 vo = (g & 1) * 16384 + (g >> 1) * 256 + c * 16;
    const u32 so = WS_O + ((tb * 4 + 2 * p) * 16 + (head >> 1)) * 1024 + (head & 1) * 512;
    ST4(of, srdS, vo, so, 0);
    const u32 ch = rowsum16(sum4(of));
    if (c == 0) ldsw<u32>(L_OCX + (hd >> 1) * 64 + ((2 * p + (g & 1)) * 4 + 2 * (hd & 1) + (g >> 1)) * 4, ch);
  }
  BAR();
  u32 soC0 = WS_OC + tb * 1024;
  asm volatile("" : "+s"(soC0));
  if (wave == 0 && lane < 16) {
    const u32 ch = ldsr<u32>(L_OCX + lane * 4) + ldsr<u32>(L_OCX + 64 + lane * 4) + tag;
    const u32 vo = lane * 4, so = soC0 + hq * 128;
    ST1(ch, srdS, vo, so, 0);
  }
  STAMP(8);
  u32x4 Bw[8][4];
  {
    const u32 base = LX + (256 * kap + 16 * (g >> 1) + 4 * (g & 1) + ((lane >> 2) & 3)) * 128 + (lane & 3) * 8;
#pragma unroll
    for (int t = 0; t < 4; ++t)
#pragma unroll
      for (int m = 0; m < 8; ++m) Bw[m][t] = trfrag(base + ((t ^ wsw) * 32) + m * 4096, 1024);
  }
  float w4[4], wsum;
  {
    const u32x4 v = ldsr<u32x4>(L_WR + lane * 16);
    float sw = (bflo(v.x) + bfhi(v.x)) + (bflo(v.y) + bfhi(v.y)) + (bflo(v.z) + bfhi(v.z)) + (bflo(v.w) + bfhi(v.w));
    sw = sum8f(sw); sw += dppf<DPP_R8>(sw);
    u32 a = f2u(sw), bb = a; swap16(a, bb); sw = u2f(a) + u2f(bb);
    a = f2u(sw); bb = a; swap32(a, bb); wsum = u2f(a) + u2f(bb);
#pragma unroll
    for (int t = 0; t < 4; ++t) w4[t] = u2f((u32)ldsr<u16>(L_WR + (64 * hq + 16 * t + c) * 2) << 16);
  }
  asm volatile("s_waitcnt vmcnt(0) lgkmcnt(0)" ::: "memory");
  STAMP(9);
  u32x4 Of[8]; u32 Oc[4];
  const u32 soO = WS_O + ((tb * 4 + rho) * 16 + 8 * kap) * 1024, soO2 = soO + 4096, soC = soC0 + 512 * kap, voC = rho * 16 + g * 4;
  LD4(Of[0], srdS, vo16, soO, 0);     LD4(Of[1], srdS, vo16, soO, 1024);  LD1(Oc[0], srdS, voC, soC, 0);
  LD4(Of[2], srdS, vo16, soO, 2048);  LD4(Of[3], srdS, vo16, soO, 3072);  LD1(Oc[1], srdS, voC, soC, 128);
  LD4(Of[4], srdS, vo16, soO2, 0);    LD4(Of[5], srdS, vo16, soO2, 1024); LD1(Oc[2], srdS, voC, soC, 256);
  LD4(Of[6], srdS, vo16, soO2, 2048); LD4(Of[7], srdS, vo16, soO2, 3072); LD1(Oc[3], srdS, voC, soC, 384);
  {
    const u32 has = tb < 28;
    const srd_t srdG = mksrd(wg, has ? DM * FF * 2 : 0), srdU = mksrd(wu, has ? DM * FF * 2 : 0), srdD = mksrd(wd, has ? DM * FF * 2 : 0);
    const u32 sx = (s8 ^ ((rr & 2) | ((wave & 1) << 2))) * 16;
    const u32 vg = rr * (FF * 2) + sx, vd = rr * 1024 + sx;
    const u32 sg = (64 * hq + 8 * wave) * (FF * 2) + tb * 128, sd = (64 * tb + 8 * wave) * 1024 + hq * 128;
    DMA(srdG, vg, sg, L_FW + wave * 1024); DMA(srdU, vg, sg, L_FW + 8192 + wave * 1024); DMA(srdD, vd, sd, L_FW + 16384 + wave * 1024);
  }
  f32x4 ah[4];
#pragma unroll
  for (int t = 0; t < 4; ++t) ah[t] = kap ? (f32x4){0.f, 0.f, res[t][0], res[t][1]} : (f32x4){res[t][0], res[t][1], 0.f, 0.f};
#define CHKO(j, SO, I0, I1, IC) do { for (u32 sp_ = 0; sp_ < SPIN_CAP; ++sp_) { const u32 ch_ = rowsum16(sum4(Of[2 * j]) + sum4(Of[2 * j + 1])) + tag; if (__all(ch_ == Oc[j])) break; \
    __builtin_amdgcn_s_sleep(1); \
    asm volatile("buffer_load_dwordx4 %0, %3, %5, %6 offen offset:" #I0 " sc1\n\tbuffer_load_dwordx4 %1, %3, %5, %6 offen offset:" #I1 " sc1\n\tbuffer_load_dword %2, %4, %5, %7 offen offset:" #IC " sc1\n\ts_waitcnt vmcnt(0)" \
                 : "+v"(Of[2 * j]), "+v"(Of[2 * j + 1]), "+v"(Oc[j]) : "v"(vo16), "v"(voC), "s"(srdS), "s"(SO), "s"(soC) : "memory"); } \
    _Pragma("unroll") for (int t = 0; t < 4; ++t) ah[t] = mfma16(Of[2 * j], Bw[2 * j][t], ah[t]); \
    _Pragma("unroll") for (int t = 0; t < 4; ++t) ah[t] = mfma16(Of[2 * j + 1], Bw[2 * j + 1][t], ah[t]); } while (0)
  WAIT3(12, Of[0], Of[1], Oc[0]); CHKO(0, soO, 0, 1024, 0);
  WAIT3(9, Of[2], Of[3], Oc[1]);  CHKO(1, soO, 2048, 3072, 128);
  WAIT3(6, Of[4], Of[5], Oc[2]);  CHKO(2, soO2, 0, 1024, 256);
  WAIT3(3, Of[6], Of[7], Oc[3]);  CHKO(3, soO2, 2048, 3072, 384);
  STAMP(10);
  float h[4][2];
  {
    const u32 a = L_PX + wave * 2048 + lane * 16;
    f32x4 s0, s1v;
#pragma unroll
    for (int t = 0; t < 4; ++t) { s0[t] = kap ? ah[t][0] : ah[t][2]; s1v[t] = kap ? ah[t][1] : ah[t][3]; h[t][0] = kap ? ah[t][2] : ah[t][0]; h[t][1] = kap ? ah[t][3] : ah[t][1]; }
    ldsw<f32x4>(a, s0); ldsw<f32x4>(a + 1024, s1v);
    BAR();
    const u32 a2 = L_PX + (wave ^ 4) * 2048 + lane * 16;
    const f32x4 u0 = ldsr<f32x4>(a2), u1 = ldsr<f32x4>(a2 + 1024);
#pragma unroll
    for (int t = 0; t < 4; ++t) { h[t][0] += u0[t]; h[t][1] += u1[t]; }
  }
  float mu, rs, rsc;
  {
    float a1[2], a2[2], a3[2];
#pragma unroll
    for (int e = 0; e < 2; ++e) {
      a1[e] = (h[0][e] + h[1][e]) + (h[2][e] + h[3][e]);
      a2[e] = __builtin_fmaf(h[0][e], h[0][e], h[1][e] * h[1][e]) + __builtin_fmaf(h[2][e], h[2][e], h[3][e] * h[3][e]);
      a3[e] = __builtin_fmaf(h[0][e], w4[0], h[1][e] * w4[1]) + __builtin_fmaf(h[2][e], w4[2], h[3][e] * w4[3]);
    }
    const bool up = c >= 8;
    const float t1 = sum8f((up ? a1[1] : a1[0]) + dppf<DPP_R8>(up ? a1[0] : a1[1]));
    const float t2 = sum8f((up ? a2[1] : a2[0]) + dppf<DPP_R8>(up ? a2[0] : a2[1]));
    const float t3 = sum8f((up ? a3[1] : a3[0]) + dppf<DPP_R8>(up ? a3[0] : a3[1]));
    const u32x4 msg = {f2u(t1), f2u(t2), f2u(t3), f2u(t1) + f2u(t2) + f2u(t3) + tag};
    const u32 soS = WS_ST + (tb * 8 + wave) * 1024, vs = hq * 128 + rr * 16, vl = s8 * 128 + rr * 16;
    if (s8 == 0) ST4(msg, srdS, vs, soS, 0);
    STAMP(11);
    u32x4 v;
    for (u32 sp = 0; sp < SPIN_CAP; ++sp) {
      __builtin_amdgcn_s_sleep(2);
      asm volatile("buffer_load_dwordx4 %0, %1, %2, %3 offen sc1\n\ts_waitcnt vmcnt(0)" : "=&v"(v) : "v"(vl), "s"(srdS), "s"(soS) : "memory");
      if (__all(v.x + v.y + v.z + tag == v.w)) break;
    }
    STAMP(12);
    mu = sum8f(u2f(v.x)) * (1.f / DM);
    const float var = __builtin_fmaxf(__builtin_fmaf(-mu, mu, sum8f(u2f(v.y)) * (1.f / DM)), 0.f);
    rs = __builtin_amdgcn_rsqf(var + 1e-5f);
    rsc = rs * __builtin_fmaf(-mu, wsum, sum8f(u2f(v.z)));
  }
  {
    const u32 row = 16 * rho + 4 * g + 2 * kap;
    const u32 rb = f2u(rsc);
    const u32 key = ((rb ^ (((int)rb >> 31) | 0x80000000u)) & ~63u) | (63u - (row + (c >> 3)));
    if (s8 == 0) ldsw<u32>(L_KEY + (row + (c >> 3)) * 4, key);
    const bool up = c >= 8;
    const float muo = dppf<DPP_R8>(mu), rso = dppf<DPP_R8>(rs);
#pragma unroll
    for (int e = 0; e < 2; ++e) {
      const float m_ = (up == (e == 1)) ? mu : muo, r_ = (up == (e == 1)) ? rs : rso;
#pragma unroll
      for (int t = 0; t < 4; ++t) {
        const float ln = (h[t][e] - m_) * r_;
        const u32 pr = pk(ln, h[t][e] + ln);
        ldsw<u16>(L_LN + (row + e) * 128 + (16 * t + c) * 2, (u16)pr);
        ldsw<u16>(L_OUT + (row + e) * 128 + (16 * t + c) * 2, (u16)(pr >> 16));
      }
    }
  }
  BAR();
  u32 key = ldsr<u32>(L_KEY + lane * 4);
  key = max(key, dppu<DPP_X1>(key)); key = max(key, dppu<DPP_X2>(key)); key = max(key, dppu<DPP_HM>(key)); key = max(key, dppu<DPP_R8>(key));
  { u32 a = key, bb = key; swap16(a, bb); key = max(a, bb); a = key; bb = key; swap32(a, bb); key = max(a, bb); }
  const u32 crow = 63u - (key & 63u);
  const srd_t srdOut = mksrd((char*)out + (size_t)tb * 65536 + hq * 128, 65536 - hq * 128);
  if (wave == 0 && lane < 16) {
    const u32x2 d = ldsr<u32x2>(L_LN + crow * 128 + lane * 8);
    const u32x4 msg = {d.x, d.y, key, d.x + d.y + key + tag};
    const u32 so = WS_CAND + (hq * 32 + tb) * 256;
    ST4(msg, srdS, vo16, so, 0);
  }
  STAMP(13);
  {
    const u32 row = 8 * wave + rr;
    const u32x4 v = ldsr<u32x4>(L_OUT + row * 128 + s8 * 16);
    if (row != crow) __builtin_amdgcn_raw_buffer_store_b128(v, srdOut, row * 1024 + s8 * 16, 0, 0);
  }
  if (wave == 0) {
    u32x4 Wg[2][4], Wu[2][4], Wd[2][4];
#pragma unroll
    for (int t = 0; t < 4; ++t)
#pragma unroll
      for (int ks = 0; ks < 2; ++ks) {
        const u32 a = L_FW + wtr + ((t ^ wsw) * 32) + ks * 4096;
        Wg[ks][t] = trfrag(a, 512); Wu[ks][t] = trfrag(a + 8192, 512); Wd[ks][t] = trfrag(a + 16384, 512);
      }
    u32x4 m[8];
    const u32 soA = WS_CAND + hq * 8192, soB = soA + 4096;
#define LD8(SA, SB) asm volatile("buffer_load_dwordx4 %0, %8, %9, %10 offen sc1\n\tbuffer_load_dwordx4 %1, %8, %9, %10 offen offset:1024 sc1\n\t" \
      "buffer_load_dwordx4 %2, %8, %9, %10 offen offset:2048 sc1\n\tbuffer_load_dwordx4 %3, %8, %9, %10 offen offset:3072 sc1\n\t" \
      "buffer_load_dwordx4 %4, %8, %9, %11 offen sc1\n\tbuffer_load_dwordx4 %5, %8, %9, %11 offen offset:1024 sc1\n\t" \
      "buffer_load_dwordx4 %6, %8, %9, %11 offen offset:2048 sc1\n\tbuffer_load_dwordx4 %7, %8, %9, %11 offen offset:3072 sc1\n\ts_waitcnt vmcnt(0)" \
      : "=&v"(m[0]), "=&v"(m[1]), "=&v"(m[2]), "=&v"(m[3]), "=&v"(m[4]), "=&v"(m[5]), "=&v"(m[6]), "=&v"(m[7]) : "v"(vo16), "s"(srdS), "s"(SA), "s"(SB) : "memory")
    for (u32 sp = 0; sp < SPIN_CAP; ++sp) {
      __builtin_amdgcn_s_sleep(2);
      LD8(soA, soB);
      bool ok = true;
#pragma unroll
      for (int i = 0; i < 8; ++i) ok &= m[i].x + m[i].y + m[i].z + tag == m[i].w;
      if (__all(ok)) break;
    }
    STAMP(14);
    u32 bk = 0, d0 = 0, d1 = 0;
#pragma unroll
    for (int i = 0; i < 8; ++i) {
      const u32 k_ = (m[i].z & ~63u) | (63u - (4 * i + g));
      const bool gt = k_ > bk;
      bk = gt ? k_ : bk; d0 = gt ? m[i].x : d0; d1 = gt ? m[i].y : d1;
    }
    u32 gk;
    { u32 a = bk, bb = bk; swap16(a, bb); gk = max(a, bb); a = gk; bb = gk; swap32(a, bb); gk = max(a, bb); }
    if (bk == gk) ldsw<u32x2>(L_VEC + c * 8, (u32x2){d0, d1});
    const u32 wtb = 63u - (__builtin_amdgcn_readfirstlane(gk) & 63u);
    const f32x4 z4 = {};
    if (tb < 28) {
      const u32x4 xa0 = ldsr<u32x4>(L_VEC + g * 16), xa1 = ldsr<u32x4>(L_VEC + 64 + g * 16);
      float gp = 0.f, upv = 0.f;
#pragma unroll
      for (int t = 0; t < 4; ++t) {
        const f32x4 dg = mfma16(xa1, Wg[1][t], mfma16(xa0, Wg[0][t], z4)), du = mfma16(xa1, Wu[1][t], mfma16(xa0, Wu[0][t], z4));
        gp = g == (u32)t ? dg[0] : gp; upv = g == (u32)t ? du[0] : upv;
      }
      const u32x4 msg = {f2u(gp), f2u(upv), 0u, f2u(gp) + f2u(upv) + tag};
      const u32 so = WS_F1 + (tb * 8 + hq) * 1024;
      ST4(msg, srdS, vo16, so, 0);
    }
    STAMP(15);
    const u32 cbase = ldsr<u32>(L_OUT + crow * 128 + (lane & 31) * 4);
    if (wtb != tb && lane < 32) __builtin_amdgcn_raw_buffer_store_b32(cbase, srdOut, crow * 1024 + lane * 4, 0, 0);
    if (tb < 28) {
      const u32 sA = WS_F1 + tb * 8192, sB = sA + 4096;
      for (u32 sp = 0; sp < SPIN_CAP; ++sp) {
        __builtin_amdgcn_s_sleep(2);
        LD8(sA, sB);
        bool ok = true;
#pragma unroll
        for (int i = 0; i < 8; ++i) ok &= m[i].x + m[i].y + m[i].z + tag == m[i].w;
        if (__all(ok)) break;
      }
      STAMP(16);
      const float gs = ((u2f(m[0].x) + u2f(m[1].x)) + (u2f(m[2].x) + u2f(m[3].x))) + ((u2f(m[4].x) + u2f(m[5].x)) + (u2f(m[6].x) + u2f(m[7].x)));
      const float us = ((u2f(m[0].y) + u2f(m[1].y)) + (u2f(m[2].y) + u2f(m[3].y))) + ((u2f(m[4].y) + u2f(m[5].y)) + (u2f(m[6].y) + u2f(m[7].y)));
      const float gu = gs * __builtin_amdgcn_rcpf(1.f + __builtin_amdgcn_exp2f(gs * -1.4426950408889634f)) * us;
      ldsw<u16>(L_VEC + lane * 2, (u16)pk(gu, gu));
      const u32x4 ga0 = ldsr<u32x4>(L_VEC + g * 16), ga1 = ldsr<u32x4>(L_VEC + 64 + g * 16);
      float y = 0.f;
#pragma unroll
      for (int t = 0; t < 4; ++t) { const f32x4 dy = mfma16(ga1, Wd[1][t], mfma16(ga0, Wd[0][t], z4)); y = g == (u32)t ? dy[0] : y; }
      const u32x2 msg = {f2u(y), f2u(y) + tag};
      const u32 vo8 = lane * 8, so = WS_F2 + (hq * 28 + tb) * 512;
      ST2(msg, srdS, vo8, so, 0);
    }
    STAMP(17);
    if (wtb == tb) {
      float y0, y1;
      const u32 sA = WS_F2 + hq * (28 * 512);
      for (u32 sp = 0; sp < SPIN_CAP; ++sp) {
        __builtin_amdgcn_s_sleep(2);
        bool ok = true; y0 = 0.f; y1 = 0.f;
        u32x4 n[6];
        const u32 sC = sA + 8192, sD = sA + 12288;
        LD4(n[0], srdS, vo16, sC, 0); LD4(n[1], srdS, vo16, sC, 1024); LD4(n[2], srdS, vo16, sC, 2048); LD4(n[3], srdS, vo16, sC, 3072);
        LD4(n[4], srdS, vo16, sD, 0); LD4(n[5], srdS, vo16, sD, 1024);
        const u32 sB = sA + 4096;
        LD8(sA, sB);
        asm volatile("s_waitcnt vmcnt(0)" : "+v"(n[0]), "+v"(n[1]), "+v"(n[2]), "+v"(n[3]), "+v"(n[4]), "+v"(n[5]));
#pragma unroll
        for (int i = 0; i < 14; ++i) { const u32x4 v = i < 8 ? m[i & 7] : n[(i - 8) % 6]; ok &= (v.x + tag == v.y) & (v.z + tag == v.w); y0 += u2f(v.x); y1 += u2f(v.z); }
        if (__all(ok)) break;
      }
      STAMP(18);
      { u32 a = f2u(y0), bb = a; swap32(a, bb); y0 = u2f(a) + u2f(bb); a = f2u(y1); bb = a; swap32(a, bb); y1 = u2f(a) + u2f(bb); }
      const u32 kb = (key & ~63u) | 32u;
      const float rsel = u2f(kb ^ ((kb >> 31) ? 0x80000000u : 0xffffffffu));
      const float sg = __builtin_amdgcn_rcpf(1.f + __builtin_amdgcn_exp2f(rsel * -1.4426950408889634f));
      const u32 res2 = pk(__builtin_fmaf(y0, sg, bflo(cbase)), __builtin_fmaf(y1, sg, bfhi(cbase)));
      if (lane < 32) __builtin_amdgcn_raw_buffer_store_b32(res2, srdOut, crow * 1024 + lane * 4, 0, 0);
      if (hq == 0 && lane == 0) { const u32 e1 = epoch + 1u; ST1(e1, srdS, zero, zero, 0); }
      if (hq == 0) {
        STAMP(19);
        u32 mn = ~0u, mx = 0;
        for (int i = 0; i < 4; ++i) { u32 v = __hip_atomic_load((__attribute__((address_space(1))) u32*)(ws + WS_DBG + (i * 64 + lane) * 4), __ATOMIC_RELAXED, __HIP_MEMORY_SCOPE_AGENT); v = stamp[19] - v; mn = min(mn, v); mx = max(mx, v); }
        for (int o = 32; o >= 1; o >>= 1) { mn = min(mn, (u32)__shfl_xor((int)mn, o)); mx = max(mx, (u32)__shfl_xor((int)mx, o)); }
        u32 d = PROBE == 100 ? mx : PROBE == 101 ? mn : PROBE == 102 ? (u32)((__builtin_readcyclecounter() - cyc0) / 24) : PROBE == 103 ? 0 : stamp[PROBE % 20] - stamp[0];
        d = min(__builtin_amdgcn_readfirstlane(d), 100000u) * 10;
        while ((u32)__builtin_amdgcn_s_memrealtime() - stamp[19] < d) __builtin_amdgcn_s_sleep(1);
      }
    }
  }
}

extern "C" void kernel_launch(void* const* d_in, const int* in_sizes, int n_in, void* d_out, int out_size, void* d_ws, size_t ws_size,
                              hipStream_t stream) {
  fused_block<<<256, 512, 0, stream>>>((const u16*)d_in[0], (const u16*)d_in[1], (const u16*)d_in[2], (const u16*)d_in[4], (const u16*)d_in[5],
                                       (const u16*)d_in[6], (const u16*)d_in[7], (u16*)d_out, (char*)d_ws);
}
```

```cpp
#include <hip/hip_runtime.h>
#include <stdint.h>
typedef unsigned short u16;
#define STAMPS 0
constexpr unsigned WS_DBG = 16777216u;
#define WS_BASE(sz) 0

__global__ __launch_bounds__(512, 2) void fused_block(const u16* x, const u16* wqkv, const u16* wo, const u16* wr, const u16* wg, const u16* wu,
                                                      const u16* wd, u16* out, char* ws) {
  __shared__ __attribute__((aligned(16))) char lds_[163840];
#if defined(__HIP_DEVICE_COMPILE__)
  asm volatile(R"ASM(
.Lfb_begin:
; ==== entry: kernel arguments straight into the descriptors
  s_load_dwordx2 s[4:5], s[0:1], 0
  s_load_dwordx2 s[8:9], s[0:1], 8
  s_load_dwordx2 s[36:37], s[0:1], 64
  s_load_dwordx2 s[16:17], s[0:1], 24
  s_getpc_b64 s[64:65]
  v_readfirstlane_b32 s44, v0
  v_and_b32 v1, 63, v0
  s_bfe_u32 s55, s2, 0x20001
  s_bfe_u32 s56, s2, 0x30003
  s_lshl3_add_u32 s47, s55, s56
  s_and_b32 s57, s2, 1
  s_lshr_b32 s58, s2, 6
  s_lshl2_add_u32 s48, s57, s58
  s_lshr_b32 s44, s44, 6
  s_and_b32 s45, s44, 3
  s_lshr_b32 s46, s44, 2
  s_add_u32 s49, s47, s48
  s_lshl2_add_u32 s49, s49, s55
  s_lshl1_add_u32 s49, s57, s49
  s_and_b32 s49, s49, 7
  s_lshl_b32 s52, s45, 11
  s_mov_b32 s6, 2097152
  s_mov_b32 s7, 0x20000
  s_mov_b32 s10, 1572864
  s_mov_b32 s11, 0x20000
  s_cmp_eq_u32 s46, 0
  s_cbranch_scc1 .Lfb_kv
; ==== waves 4-7: stream x / Wk / Wv / Wq into LDS, then the Q projection
  v_lshrrev_b32 v148, 3, v1
  v_and_b32 v149, 7, v1
  v_lshrrev_b32 v150, 4, v1
  v_xor_b32 v150, v150, v149
  s_lshl2_add_u32 s55, s47, s45
  s_lshl_b32 s55, s55, 14
  v_lshl_add_u32 v140, v148, 10, s55
  v_lshl_add_u32 v140, v150, 4, v140
  s_movk_i32 s56, 7168
  v_xad_u32 v141, v140, 64, s56
  v_bitop3_b32 v150, v149, v148, 2 bitop3:0x78
  s_mul_i32 s55, s45, 49152
  s_lshl_b32 s56, s48, 7
  s_add_u32 s55, s55, s56
  v_mul_u32_u24 v146, 3072, v148
  v_lshl_add_u32 v146, v150, 4, v146
  v_add_u32 v146, s55, v146
  s_movk_i32 s56, 23552
  v_xad_u32 v147, v146, 64, s56
  v_add_u32 v142, 1024, v146
  v_add_u32 v143, 1024, v147
  v_add_u32 v144, 2048, v146
  v_add_u32 v145, 2048, v147
  s_add_u32 s76, s49, 0
  s_and_b32 s76, s76, 7
  s_add_u32 m0, s52, 0
  s_lshl_b32 s76, s76, 7
  s_waitcnt lgkmcnt(0)
  buffer_load_dwordx4 v140, s[4:7], s76 offen lds
  buffer_load_dwordx4 v141, s[4:7], s76 offen offset:1024 lds
  s_add_u32 s76, s49, 0
  s_and_b32 s76, s76, 7
  s_add_u32 m0, s52, 65536
  s_mul_i32 s76, s76, 196608
  buffer_load_dwordx4 v142, s[8:11], s76 offen lds
  buffer_load_dwordx4 v143, s[8:11], s76 offen offset:1024 lds
  s_add_u32 s76, s49, 0
  s_and_b32 s76, s76, 7
  s_add_u32 m0, s52, 73728
  s_mul_i32 s76, s76, 196608
  buffer_load_dwordx4 v144, s[8:11], s76 offen lds
  buffer_load_dwordx4 v145, s[8:11], s76 offen offset:1024 lds
  s_add_u32 s76, s49, 1
  s_and_b32 s76, s76, 7
  s_add_u32 m0, s52, 8192
  s_lshl_b32 s76, s76, 7
  buffer_load_dwordx4 v140, s[4:7], s76 offen lds
  buffer_load_dwordx4 v141, s[4:7], s76 offen offset:1024 lds
  s_load_dword s51, s[36:37], 0
  s_load_dwordx2 s[12:13], s[0:1], 16
  s_load_dwordx2 s[20:21], s[0:1], 32
  s_load_dwordx2 s[24:25], s[0:1], 40
  s_load_dwordx2 s[28:29], s[0:1], 48
  s_load_dwordx2 s[32:33], s[0:1], 56
  s_mov_b32 s38, 7995392
  s_mov_b32 s14, 524288
  s_mov_b32 s34, 2097152
  s_cmp_lt_u32 s47, 28
  s_cselect_b32 s22, 1835008, 0
  s_mov_b32 s26, s22
  s_mov_b32 s30, s22
  s_mov_b32 s39, 0x20000
  s_mov_b32 s15, 0x20000
  s_mov_b32 s35, 0x20000
  s_mov_b32 s23, 0x20000
  s_mov_b32 s27, 0x20000
  s_mov_b32 s31, 0x20000
  s_mov_b32 s43, 0x20000
  s_mov_b64 s[40:41], s[36:37]
  s_mov_b32 s66, 0xffff
  s_mov_b32 s67, 0xffff
  s_mov_b32 s54, 0x40000
  s_lshl_b32 s63, s44, 10
  s_lshl2_add_u32 s59, s48, s45
  s_lshl_b32 s60, s47, 5
  s_add_u32 s60, s60, s59
  s_lshl1_add_u32 s60, s60, s46
  s_mul_i32 s60, s60, 2304
  s_add_u32 s53, s60, 4096
  s_lshl1_add_u32 s60, s47, s46
  s_lshl_b32 s60, s60, 6
  s_add_u32 s60, s60, s59
  s_lshl_b32 s60, s60, 9
  s_add_u32 s62, s60, 5242880
  s_and_b32 s59, s47, 7
  s_and_b32 s60, s48, 3
  s_xor_b32 s60, s60, s59
  s_lshl_b32 s75, s60, 16
  s_lshl_b32 s59, s59, 13
  s_or_b32 s74, s59, s63
  v_lshlrev_b32 v2, 4, v1
  v_lshlrev_b32 v3, 2, v1
  v_and_b32 v228, 32, v1
  v_and_b32 v4, 0xf0, v2
  v_lshl_or_b32 v4, v228, 3, v4
  v_lshrrev_b32 v228, 3, v1
  v_and_b32 v229, 7, v1
  v_bitop3_b32 v230, v229, v228, 6 bitop3:0x78
  s_lshl_b32 s55, s44, 13
  s_lshl_b32 s56, s48, 7
  s_add_u32 s55, s55, s56
  v_lshl_add_u32 v5, v228, 10, s55
  v_lshl_add_u32 v5, v230, 4, v5
  v_bitop3_b32 v230, v229, v228, 2 bitop3:0x78
  s_and_b32 s57, s44, 1
  s_lshl_b32 s57, s57, 2
  v_xor_b32 v230, s57, v230
  s_lshl_b32 s57, s47, 16
  s_add_u32 s55, s55, s57
  v_lshl_add_u32 v15, v228, 10, s55
  v_lshl_add_u32 v15, v230, 4, v15
  s_lshl3_add_u32 s55, s48, s44
  s_mul_i32 s55, s55, 28672
  s_lshl_b32 s57, s47, 7
  s_add_u32 s55, s55, s57
  v_mul_u32_u24 v14, 3584, v228
  v_lshl_add_u32 v14, v230, 4, v14
  v_add_u32 v14, s55, v14
  s_mul_i32 s55, s45, 2304
  s_add_u32 s76, s49, 1
  s_and_b32 s76, s76, 7
  s_add_u32 m0, s52, 81920
  s_mul_i32 s76, s76, 196608
  buffer_load_dwordx4 v142, s[8:11], s76 offen lds
  buffer_load_dwordx4 v143, s[8:11], s76 offen offset:1024 lds
  s_add_u32 s56, s55, 74752
  s_add_u32 s55, s55, 65536
  v_add_u32 v6, s56, v2
  v_add_u32 v8, s56, v3
  v_add_u32 v7, s55, v2
  v_add_u32 v9, s55, v3
  v_and_b32 v228, 16, v1
  v_lshl_or_b32 v10, v228, 10, v4
  v_and_b32 v229, 32, v1
  v_lshl_or_b32 v229, v228, 3, v229
  s_add_u32 s76, s49, 1
  s_and_b32 s76, s76, 7
  s_add_u32 m0, s52, 90112
  s_mul_i32 s76, s76, 196608
  buffer_load_dwordx4 v144, s[8:11], s76 offen lds
  buffer_load_dwordx4 v145, s[8:11], s76 offen offset:1024 lds
  v_and_b32 v230, 14, v1
  v_lshl_or_b32 v229, v230, 1, v229
  s_lshr_b32 s55, s45, 1
  s_lshl_b32 s55, s55, 9
  s_and_b32 s56, s45, 1
  s_lshl_b32 s56, s56, 6
  s_add_u32 s55, s55, s56
  s_add_u32 s55, s55, 161536
  v_add_u32 v11, s55, v229
  v_xor_b32 v12, 32, v2
  s_add_u32 s76, s49, 2
  s_and_b32 s76, s76, 7
  s_add_u32 m0, s52, 16384
  s_lshl_b32 s76, s76, 7
  buffer_load_dwordx4 v140, s[4:7], s76 offen lds
  buffer_load_dwordx4 v141, s[4:7], s76 offen offset:1024 lds
  v_lshrrev_b32 v13, 3, v12
  v_and_b32 v13, 0x7c, v13
  v_bfe_u32 v228, v1, 2, 3
  v_and_b32 v229, 32, v1
  v_lshlrev_b32 v229, 6, v229
  v_lshl_or_b32 v229, v228, 7, v229
  v_and_b32 v228, 3, v1
  v_lshl_or_b32 v229, v228, 3, v229
  v_and_b32 v228, 24, v1
  v_lshl_or_b32 v229, v228, 2, v229
  s_add_u32 s76, s49, 2
  s_and_b32 s76, s76, 7
  s_add_u32 m0, s52, 98304
  s_mul_i32 s76, s76, 196608
  buffer_load_dwordx4 v142, s[8:11], s76 offen lds
  buffer_load_dwordx4 v143, s[8:11], s76 offen offset:1024 lds
  v_xor_b32 v24, 32768, v229
  v_xor_b32 v25, 32800, v229
  v_xor_b32 v26, 32832, v229
  v_xor_b32 v27, 32864, v229
  s_sub_u32 s55, s48, s49
  s_and_b32 s55, s55, 7
  s_lshl_b32 s55, s55, 13
  s_add_u32 s55, s55, s52
  s_add_u32 s55, s55, 256
  v_lshrrev_b32 v228, 3, v1
  s_waitcnt vmcnt(8)
  s_barrier
  s_add_u32 s76, s49, 2
  s_and_b32 s76, s76, 7
  s_add_u32 m0, s52, 106496
  s_mul_i32 s76, s76, 196608
  buffer_load_dwordx4 v144, s[8:11], s76 offen lds
  buffer_load_dwordx4 v145, s[8:11], s76 offen offset:1024 lds
  v_xor_b32 v228, 1, v228
  v_and_b32 v229, 48, v1
  v_lshlrev_b32 v229, 5, v229
  v_lshl_or_b32 v229, v228, 4, v229
  v_and_b32 v228, 7, v1
  v_lshl_or_b32 v229, v228, 1, v229
  v_add_u32 v17, s55, v229
  s_add_u32 s55, s52, 114688
  v_lshl_add_u32 v18, v1, 3, s55
  v_add_u32 v19, -8192, v18
  s_add_u32 s76, s49, 3
  s_and_b32 s76, s76, 7
  s_add_u32 m0, s52, 24576
  s_lshl_b32 s76, s76, 7
  buffer_load_dwordx4 v140, s[4:7], s76 offen lds
  buffer_load_dwordx4 v141, s[4:7], s76 offen offset:1024 lds
  s_lshl_b32 s55, s48, 7
  v_lshl_add_u32 v20, v1, 1, s55
  v_and_b32 v228, 15, v1
  s_add_u32 s55, s55, 160256
  v_lshl_add_u32 v21, v228, 1, s55
  s_mul_i32 s59, s47, 43
  s_lshr_b32 s59, s59, 7
  s_mul_i32 s59, s59, 3
  s_sub_u32 s59, s47, s59
  s_cmp_eq_u32 s59, 0
  s_cselect_b32 s59, 3, s59
  s_add_u32 s76, s49, 3
  s_and_b32 s76, s76, 7
  s_add_u32 m0, s52, 114688
  s_mul_i32 s76, s76, 196608
  buffer_load_dwordx4 v142, s[8:11], s76 offen lds
  buffer_load_dwordx4 v143, s[8:11], s76 offen offset:1024 lds
  s_mul_i32 s60, s59, 147456
  s_sub_u32 s60, s53, s60
  s_cmp_ge_u32 s47, s59
  s_cselect_b32 s70, s60, 0
  s_sub_u32 s59, s59, 1
  s_cmp_eq_u32 s59, 0
  s_cselect_b32 s59, 3, s59
  s_mul_i32 s60, s59, 147456
  s_sub_u32 s60, s53, s60
  s_cmp_ge_u32 s47, s59
  s_cselect_b32 s71, s60, 0
  s_sub_u32 s59, s59, 1
  s_cmp_eq_u32 s59, 0
  s_cselect_b32 s59, 3, s59
  s_waitcnt vmcnt(8)
  s_barrier
  s_add_u32 s76, s49, 3
  s_and_b32 s76, s76, 7
  s_add_u32 m0, s52, 122880
  s_mul_i32 s76, s76, 196608
  buffer_load_dwordx4 v144, s[8:11], s76 offen lds
  buffer_load_dwordx4 v145, s[8:11], s76 offen offset:1024 lds
  s_mul_i32 s60, s59, 147456
  s_sub_u32 s60, s53, s60
  s_cmp_ge_u32 s47, s59
  s_cselect_b32 s72, s60, 0
  s_mov_b32 s59, 4
  s_mul_i32 s60, s59, 147456
  s_sub_u32 s60, s53, s60
  s_cmp_ge_u32 s47, s59
  s_cselect_b32 s73, s60, 0
  v_lshrrev_b32 v228, 3, v1
  v_and_b32 v229, 31, v1
  v_and_b32 v228, 4, v228
  s_add_u32 s76, s49, 4
  s_and_b32 s76, s76, 7
  s_add_u32 m0, s52, 32768
  s_lshl_b32 s76, s76, 7
  buffer_load_dwordx4 v140, s[4:7], s76 offen lds
  buffer_load_dwordx4 v141, s[4:7], s76 offen offset:1024 lds
  v_sub_u32 v16, v229, v228
  v_lshlrev_b32_e64 v229, v229, -2
  v_not_b32 v229, v229
  v_lshrrev_b32 v229, v228, v229
  v_bfe_i32 v100, v229, 0, 1
  v_bfe_i32 v101, v229, 1, 1
  v_bfe_i32 v102, v229, 2, 1
  v_bfe_i32 v103, v229, 3, 1
  v_bfe_i32 v104, v229, 8, 1
  v_bfe_i32 v105, v229, 9, 1
  s_add_u32 s76, s49, 4
  s_and_b32 s76, s76, 7
  s_add_u32 m0, s52, 131072
  s_mul_i32 s76, s76, 196608
  buffer_load_dwordx4 v142, s[8:11], s76 offen lds
  buffer_load_dwordx4 v143, s[8:11], s76 offen offset:1024 lds
  v_bfe_i32 v106, v229, 10, 1
  v_bfe_i32 v107, v229, 11, 1
  v_bfe_i32 v108, v229, 16, 1
  v_bfe_i32 v109, v229, 17, 1
  v_bfe_i32 v110, v229, 18, 1
  v_bfe_i32 v111, v229, 19, 1
  v_bfe_i32 v112, v229, 24, 1
  v_bfe_i32 v113, v229, 25, 1
  v_bfe_i32 v114, v229, 26, 1
  v_bfe_i32 v115, v229, 27, 1
  s_waitcnt vmcnt(8)
  s_barrier
  s_add_u32 s76, s49, 4
  s_and_b32 s76, s76, 7
  s_add_u32 m0, s52, 139264
  s_mul_i32 s76, s76, 196608
  buffer_load_dwordx4 v144, s[8:11], s76 offen lds
  buffer_load_dwordx4 v145, s[8:11], s76 offen offset:1024 lds
  v_bfi_b32 v116, s66, v100, v101
  v_bfi_b32 v117, s66, v102, v103
  v_bfi_b32 v118, s66, v104, v105
  v_bfi_b32 v119, s66, v106, v107
  v_bfi_b32 v120, s66, v108, v109
  v_bfi_b32 v121, s66, v110, v111
  v_bfi_b32 v122, s66, v112, v113
  v_bfi_b32 v123, s66, v114, v115
  v_and_b32 v22, 27, v1
  v_mov_b32 v23, 1065369472
  s_add_u32 s76, s49, 5
  s_and_b32 s76, s76, 7
  s_add_u32 m0, s52, 40960
  s_lshl_b32 s76, s76, 7
  buffer_load_dwordx4 v140, s[4:7], s76 offen lds
  buffer_load_dwordx4 v141, s[4:7], s76 offen offset:1024 lds
  v_cmp_eq_u32 vcc, 16, v22
  s_nop 1
  v_cndmask_b32_e64 v22, 0, v23, vcc
  v_mov_b32 v23, v22
  s_cmp_lg_u32 s70, 0
  s_cselect_b32 s55, -1, 0
  v_and_b32 v64, s55, v22
  v_and_b32 v65, s55, v22
  v_mov_b64 v[66:67], v[64:65]
  v_mov_b64 v[68:69], v[64:65]
  v_mov_b64 v[70:71], v[64:65]
  s_add_u32 s76, s49, 5
  s_and_b32 s76, s76, 7
  s_add_u32 m0, s52, 65536
  s_mul_i32 s76, s76, 196608
  buffer_load_dwordx4 v142, s[8:11], s76 offen lds
  buffer_load_dwordx4 v143, s[8:11], s76 offen offset:1024 lds
  s_cmp_lg_u32 s71, 0
  s_cselect_b32 s55, -1, 0
  v_and_b32 v72, s55, v22
  v_and_b32 v73, s55, v22
  v_mov_b64 v[74:75], v[72:73]
  v_mov_b64 v[76:77], v[72:73]
  v_mov_b64 v[78:79], v[72:73]
  s_cmp_lg_u32 s72, 0
  s_cselect_b32 s55, -1, 0
  v_and_b32 v80, s55, v22
  v_and_b32 v81, s55, v22
  s_waitcnt vmcnt(8)
  s_barrier
  s_add_u32 s76, s49, 5
  s_and_b32 s76, s76, 7
  s_add_u32 m0, s52, 73728
  s_mul_i32 s76, s76, 196608
  buffer_load_dwordx4 v144, s[8:11], s76 offen lds
  buffer_load_dwordx4 v145, s[8:11], s76 offen offset:1024 lds
  v_mov_b64 v[82:83], v[80:81]
  v_mov_b64 v[84:85], v[80:81]
  v_mov_b64 v[86:87], v[80:81]
  s_cmp_lg_u32 s73, 0
  s_cselect_b32 s55, -1, 0
  v_and_b32 v56, s55, v22
  v_and_b32 v57, s55, v22
  v_mov_b64 v[58:59], v[56:57]
  v_mov_b64 v[60:61], v[56:57]
  v_mov_b64 v[62:63], v[56:57]
  s_add_u32 s76, s49, 6
  s_and_b32 s76, s76, 7
  s_add_u32 m0, s52, 49152
  s_lshl_b32 s76, s76, 7
  buffer_load_dwordx4 v140, s[4:7], s76 offen lds
  buffer_load_dwordx4 v141, s[4:7], s76 offen offset:1024 lds
  v_mov_b64 v[48:49], v[22:23]
  v_mov_b64 v[50:51], v[22:23]
  v_mov_b64 v[52:53], v[22:23]
  v_mov_b64 v[54:55], v[22:23]
  v_mov_b64 v[124:125], 0
  v_mov_b64 v[126:127], 0
  v_mov_b64 v[128:129], 0
  v_mov_b64 v[130:131], 0
  v_mov_b64 v[132:133], 0
  s_add_u32 s76, s49, 6
  s_and_b32 s76, s76, 7
  s_add_u32 m0, s52, 81920
  s_mul_i32 s76, s76, 196608
  buffer_load_dwordx4 v142, s[8:11], s76 offen lds
  buffer_load_dwordx4 v143, s[8:11], s76 offen offset:1024 lds
  v_mov_b64 v[134:135], 0
  v_mov_b64 v[136:137], 0
  v_mov_b64 v[138:139], 0
  v_bfe_u32 v232, v1, 1, 3
  v_lshrrev_b32 v233, 4, v1
  v_xor_b32 v232, v232, v233
  v_and_b32 v228, 15, v1
  v_lshlrev_b32 v228, 7, v228
  v_lshl_or_b32 v228, v232, 4, v228
  s_waitcnt vmcnt(8)
  s_barrier
  s_add_u32 s76, s49, 6
  s_and_b32 s76, s76, 7
  s_add_u32 m0, s52, 90112
  s_mul_i32 s76, s76, 196608
  buffer_load_dwordx4 v144, s[8:11], s76 offen lds
  buffer_load_dwordx4 v145, s[8:11], s76 offen offset:1024 lds
  v_xor_b32 v229, 64, v228
  v_bfe_u32 v232, v1, 2, 2
  v_lshl_or_b32 v232, v233, 3, v232
  v_and_b32 v233, 3, v1
  v_cmp_eq_u32 vcc, 0, v233
  v_lshlrev_b32 v233, 3, v233
  v_lshl_or_b32 v230, v232, 7, v233
  v_mov_b32 v233, 1065369472
  v_bfe_u32 v232, v1, 3, 2
  s_add_u32 s76, s49, 7
  s_and_b32 s76, s76, 7
  s_add_u32 m0, s52, 57344
  s_lshl_b32 s76, s76, 7
  buffer_load_dwordx4 v140, s[4:7], s76 offen lds
  buffer_load_dwordx4 v141, s[4:7], s76 offen offset:1024 lds
  v_cndmask_b32_e64 v234, 0, v233, vcc
  v_xor_b32 v232, s45, v232
  v_mov_b32 v235, v234
  v_lshl_or_b32 v230, v232, 5, v230
  v_mov_b64 v[236:237], v[234:235]
  v_add_u32 v231, 106496, v230
  v_add_u32 v230, 65536, v230
  v_and_b32 v224, 60, v3
  v_add_u32 v224, 159744, v224
  s_add_u32 s76, s49, 7
  s_and_b32 s76, s76, 7
  s_add_u32 m0, s52, 98304
  s_mul_i32 s76, s76, 196608
  buffer_load_dwordx4 v142, s[8:11], s76 offen lds
  buffer_load_dwordx4 v143, s[8:11], s76 offen offset:1024 lds
  s_add_u32 s55, s52, 98304
  v_add_u32 v225, s55, v2
  s_mul_i32 s55, s45, 3072
  s_add_u32 s55, s55, 147456
  v_add_u32 v226, s55, v2
  v_add_u32 v227, s55, v4
  s_waitcnt lgkmcnt(0)
  s_add_u32 s51, s51, 1
  s_mul_i32 s50, s51, 0x9E3779B1
  s_and_b32 s61, s51, 0xff
  s_waitcnt vmcnt(8)
  s_barrier
  s_add_u32 s76, s49, 7
  s_and_b32 s76, s76, 7
  s_add_u32 m0, s52, 106496
  s_mul_i32 s76, s76, 196608
  buffer_load_dwordx4 v144, s[8:11], s76 offen lds
  buffer_load_dwordx4 v145, s[8:11], s76 offen offset:1024 lds
  s_or_b32 s50, s50, 1
  ds_read_b128 v[168:171], v228 offset:0
  ds_read_b128 v[172:175], v228 offset:2048
  ds_read_b128 v[176:179], v228 offset:4096
  ds_read_b128 v[180:183], v228 offset:6144
  ds_read_b128 v[184:187], v229 offset:0
  ds_read_b128 v[188:191], v229 offset:2048
  ds_read_b128 v[192:195], v229 offset:4096
  ds_read_b128 v[196:199], v229 offset:6144
  s_add_u32 s76, s49, 0
  s_and_b32 s76, s76, 7
  s_add_u32 m0, s52, 114688
  s_mul_i32 s76, s76, 196608
  buffer_load_dwordx4 v146, s[8:11], s76 offen lds
  buffer_load_dwordx4 v147, s[8:11], s76 offen offset:1024 lds
  s_add_u32 s76, s49, 1
  s_and_b32 s76, s76, 7
  s_add_u32 m0, s52, 122880
  s_mul_i32 s76, s76, 196608
  buffer_load_dwordx4 v146, s[8:11], s76 offen lds
  buffer_load_dwordx4 v147, s[8:11], s76 offen offset:1024 lds
  s_waitcnt vmcnt(8)
  s_barrier
  s_add_u32 s76, s49, 2
  s_and_b32 s76, s76, 7
  s_add_u32 m0, s52, 131072
  s_mul_i32 s76, s76, 196608
  buffer_load_dwordx4 v146, s[8:11], s76 offen lds
  buffer_load_dwordx4 v147, s[8:11], s76 offen offset:1024 lds
  s_add_u32 s76, s49, 3
  s_and_b32 s76, s76, 7
  s_add_u32 m0, s52, 139264
  s_mul_i32 s76, s76, 196608
  buffer_load_dwordx4 v146, s[8:11], s76 offen lds
  buffer_load_dwordx4 v147, s[8:11], s76 offen offset:1024 lds
  s_add_u32 s76, s49, 4
  s_and_b32 s76, s76, 7
  s_add_u32 m0, s52, 65536
  s_mul_i32 s76, s76, 196608
  buffer_load_dwordx4 v146, s[8:11], s76 offen lds
  buffer_load_dwordx4 v147, s[8:11], s76 offen offset:1024 lds
  s_waitcnt vmcnt(10)
  s_barrier
  s_add_u32 s76, s49, 5
  s_and_b32 s76, s76, 7
  s_add_u32 m0, s52, 73728
  s_mul_i32 s76, s76, 196608
  buffer_load_dwordx4 v146, s[8:11], s76 offen lds
  buffer_load_dwordx4 v147, s[8:11], s76 offen offset:1024 lds
  ds_read_u16_d16_hi v124, v17 offset:0
  ds_read_u16_d16_hi v125, v17 offset:128
  v_xor_b32 v17, 32, v17
  ds_read_u16_d16_hi v128, v17 offset:0
  s_add_u32 s76, s49, 6
  s_and_b32 s76, s76, 7
  s_add_u32 m0, s52, 81920
  s_mul_i32 s76, s76, 196608
  buffer_load_dwordx4 v146, s[8:11], s76 offen lds
  buffer_load_dwordx4 v147, s[8:11], s76 offen offset:1024 lds
  ds_read_u16_d16_hi v129, v17 offset:128
  v_xor_b32 v17, 64, v17
  ds_read_u16_d16_hi v136, v17 offset:0
  ds_read_u16_d16_hi v137, v17 offset:128
  s_add_u32 s76, s49, 7
  s_and_b32 s76, s76, 7
  s_add_u32 m0, s52, 90112
  s_mul_i32 s76, s76, 196608
  buffer_load_dwordx4 v146, s[8:11], s76 offen lds
  buffer_load_dwordx4 v147, s[8:11], s76 offen offset:1024 lds
  v_xor_b32 v17, 32, v17
  ds_read_u16_d16_hi v132, v17 offset:0
  ds_read_u16_d16_hi v133, v17 offset:128
  s_waitcnt vmcnt(8)
  s_barrier
  ds_read_b64_tr_b16 v[200:201], v231 offset:8192
  ds_read_b64_tr_b16 v[202:203], v231 offset:8704
  ds_read_b64_tr_b16 v[204:205], v231 offset:12288
  ds_read_b64_tr_b16 v[206:207], v231 offset:12800
  s_waitcnt lgkmcnt(2)
  v_mfma_f32_16x16x32_bf16 v[148:151], v[200:203], v[168:171], 0
  ds_read_b128 v[168:171], v228 offset:8192
  ds_read_b64_tr_b16 v[208:209], v231 offset:16384
  v_mfma_f32_16x16x32_bf16 v[152:155], v[200:203], v[172:175], 0
  ds_read_b128 v[172:175], v228 offset:10240
  ds_read_b64_tr_b16 v[210:211], v231 offset:16896
  v_mfma_f32_16x16x32_bf16 v[156:159], v[200:203], v[176:179], 0
  ds_read_b128 v[176:179], v228 offset:12288
  ds_read_b64_tr_b16 v[212:213], v231 offset:20480
  v_mfma_f32_16x16x32_bf16 v[160:163], v[200:203], v[180:183], 0
  ds_read_b128 v[180:183], v228 offset:14336
  ds_read_b64_tr_b16 v[214:215], v231 offset:20992
  v_mfma_f32_16x16x32_bf16 v[164:167], v[200:203], v[234:237], 0
  s_waitcnt lgkmcnt(8)
  v_mfma_f32_16x16x32_bf16 v[148:151], v[204:207], v[184:187], v[148:151]
  ds_read_b128 v[184:187], v229 offset:8192
  v_mfma_f32_16x16x32_bf16 v[152:155], v[204:207], v[188:191], v[152:155]
  ds_read_b128 v[188:191], v229 offset:10240
  v_mfma_f32_16x16x32_bf16 v[156:159], v[204:207], v[192:195], v[156:159]
  ds_read_b128 v[192:195], v229 offset:12288
  v_mfma_f32_16x16x32_bf16 v[160:163], v[204:207], v[196:199], v[160:163]
  ds_read_b128 v[196:199], v229 offset:14336
  v_mfma_f32_16x16x32_bf16 v[164:167], v[204:207], v[234:237], v[164:167]
  s_waitcnt lgkmcnt(8)
  v_mfma_f32_16x16x32_bf16 v[148:151], v[208:211], v[168:171], v[148:151]
  ds_read_b128 v[168:171], v228 offset:16384
  ds_read_b64_tr_b16 v[200:201], v231 offset:24576
  v_mfma_f32_16x16x32_bf16 v[152:155], v[208:211], v[172:175], v[152:155]
  ds_read_b128 v[172:175], v228 offset:18432
  ds_read_b64_tr_b16 v[202:203], v231 offset:25088
  s_waitcnt lgkmcnt(11)
  v_mfma_f32_16x16x32_bf16 v[156:159], v[208:211], v[176:179], v[156:159]
  ds_read_b128 v[176:179], v228 offset:20480
  ds_read_b64_tr_b16 v[204:205], v231 offset:28672
  s_waitcnt lgkmcnt(11)
  v_mfma_f32_16x16x32_bf16 v[160:163], v[208:211], v[180:183], v[160:163]
  ds_read_b128 v[180:183], v228 offset:22528
  ds_read_b64_tr_b16 v[206:207], v231 offset:29184
  v_mfma_f32_16x16x32_bf16 v[164:167], v[208:211], v[234:237], v[164:167]
  s_waitcnt lgkmcnt(11)
  v_mfma_f32_16x16x32_bf16 v[148:151], v[212:215], v[184:187], v[148:151]
  ds_read_b128 v[184:187], v229 offset:16384
  s_waitcnt lgkmcnt(11)
  v_mfma_f32_16x16x32_bf16 v[152:155], v[212:215], v[188:191], v[152:155]
  ds_read_b128 v[188:191], v229 offset:18432
  s_waitcnt lgkmcnt(11)
  v_mfma_f32_16x16x32_bf16 v[156:159], v[212:215], v[192:195], v[156:159]
  ds_read_b128 v[192:195], v229 offset:20480
  s_waitcnt lgkmcnt(11)
  v_mfma_f32_16x16x32_bf16 v[160:163], v[212:215], v[196:199], v[160:163]
  ds_read_b128 v[196:199], v229 offset:22528
  v_mfma_f32_16x16x32_bf16 v[164:167], v[212:215], v[234:237], v[164:167]
  s_waitcnt lgkmcnt(8)
  v_mfma_f32_16x16x32_bf16 v[148:151], v[200:203], v[168:171], v[148:151]
  ds_read_b128 v[168:171], v228 offset:24576
  ds_read_b64_tr_b16 v[208:209], v231 offset:32768
  v_mfma_f32_16x16x32_bf16 v[152:155], v[200:203], v[172:175], v[152:155]
  ds_read_b128 v[172:175], v228 offset:26624
  ds_read_b64_tr_b16 v[210:211], v231 offset:33280
  s_waitcnt lgkmcnt(11)
  v_mfma_f32_16x16x32_bf16 v[156:159], v[200:203], v[176:179], v[156:159]
  ds_read_b128 v[176:179], v228 offset:28672
  ds_read_b64_tr_b16 v[212:213], v231 offset:36864
  s_waitcnt lgkmcnt(11)
  v_mfma_f32_16x16x32_bf16 v[160:163], v[200:203], v[180:183], v[160:163]
  ds_read_b128 v[180:183], v228 offset:30720
  ds_read_b64_tr_b16 v[214:215], v231 offset:37376
  v_mfma_f32_16x16x32_bf16 v[164:167], v[200:203], v[234:237], v[164:167]
  s_waitcnt lgkmcnt(11)
  v_mfma_f32_16x16x32_bf16 v[148:151], v[204:207], v[184:187], v[148:151]
  ds_read_b128 v[184:187], v229 offset:24576
  s_waitcnt lgkmcnt(11)
  v_mfma_f32_16x16x32_bf16 v[152:155], v[204:207], v[188:191], v[152:155]
  ds_read_b128 v[188:191], v229 offset:26624
  s_waitcnt lgkmcnt(11)
  v_mfma_f32_16x16x32_bf16 v[156:159], v[204:207], v[192:195], v[156:159]
  ds_read_b128 v[192:195], v229 offset:28672
  s_waitcnt lgkmcnt(11)
  v_mfma_f32_16x16x32_bf16 v[160:163], v[204:207], v[196:199], v[160:163]
  ds_read_b128 v[196:199], v229 offset:30720
  v_mfma_f32_16x16x32_bf16 v[164:167], v[204:207], v[234:237], v[164:167]
  s_waitcnt vmcnt(0)
  s_barrier
  s_waitcnt lgkmcnt(8)
  v_mfma_f32_16x16x32_bf16 v[148:151], v[208:211], v[168:171], v[148:151]
  ds_read_b128 v[168:171], v228 offset:32768
  ds_read_b64_tr_b16 v[200:201], v230 offset:0
  v_mfma_f32_16x16x32_bf16 v[152:155], v[208:211], v[172:175], v[152:155]
  ds_read_b128 v[172:175], v228 offset:34816
  ds_read_b64_tr_b16 v[202:203], v230 offset:512
  s_waitcnt lgkmcnt(11)
  v_mfma_f32_16x16x32_bf16 v[156:159], v[208:211], v[176:179], v[156:159]
  ds_read_b128 v[176:179], v228 offset:36864
  ds_read_b64_tr_b16 v[204:205], v230 offset:4096
  s_waitcnt lgkmcnt(11)
  v_mfma_f32_16x16x32_bf16 v[160:163], v[208:211], v[180:183], v[160:163]
  ds_read_b128 v[180:183], v228 offset:38912
  ds_read_b64_tr_b16 v[206:207], v230 offset:4608
  v_mfma_f32_16x16x32_bf16 v[164:167], v[208:211], v[234:237], v[164:167]
  s_waitcnt lgkmcnt(11)
  v_mfma_f32_16x16x32_bf16 v[148:151], v[212:215], v[184:187], v[148:151]
  ds_read_b128 v[184:187], v229 offset:32768
  s_waitcnt lgkmcnt(11)
  v_mfma_f32_16x16x32_bf16 v[152:155], v[212:215], v[188:191], v[152:155]
  ds_read_b128 v[188:191], v229 offset:34816
  s_waitcnt lgkmcnt(11)
  v_mfma_f32_16x16x32_bf16 v[156:159], v[212:215], v[192:195], v[156:159]
  ds_read_b128 v[192:195], v229 offset:36864
  s_waitcnt lgkmcnt(11)
  v_mfma_f32_16x16x32_bf16 v[160:163], v[212:215], v[196:199], v[160:163]
  ds_read_b128 v[196:199], v229 offset:38912
  v_mfma_f32_16x16x32_bf16 v[164:167], v[212:215], v[234:237], v[164:167]
  s_waitcnt lgkmcnt(8)
  v_mfma_f32_16x16x32_bf16 v[148:151], v[200:203], v[168:171], v[148:151]
  ds_read_b128 v[168:171], v228 offset:40960
  ds_read_b64_tr_b16 v[208:209], v230 offset:8192
  v_mfma_f32_16x16x32_bf16 v[152:155], v[200:203], v[172:175], v[152:155]
  ds_read_b128 v[172:175], v228 offset:43008
  ds_read_b64_tr_b16 v[210:211], v230 offset:8704
  s_waitcnt lgkmcnt(11)
  v_mfma_f32_16x16x32_bf16 v[156:159], v[200:203], v[176:179], v[156:159]
  ds_read_b128 v[176:179], v228 offset:45056
  ds_read_b64_tr_b16 v[212:213], v230 offset:12288
  s_waitcnt lgkmcnt(11)
  v_mfma_f32_16x16x32_bf16 v[160:163], v[200:203], v[180:183], v[160:163]
  ds_read_b128 v[180:183], v228 offset:47104
  ds_read_b64_tr_b16 v[214:215], v230 offset:12800
  v_mfma_f32_16x16x32_bf16 v[164:167], v[200:203], v[234:237], v[164:167]
  s_waitcnt lgkmcnt(11)
  v_mfma_f32_16x16x32_bf16 v[148:151], v[204:207], v[184:187], v[148:151]
  ds_read_b128 v[184:187], v229 offset:40960
  s_waitcnt lgkmcnt(11)
  v_mfma_f32_16x16x32_bf16 v[152:155], v[204:207], v[188:191], v[152:155]
  ds_read_b128 v[188:191], v229 offset:43008
  s_waitcnt lgkmcnt(11)
  v_mfma_f32_16x16x32_bf16 v[156:159], v[204:207], v[192:195], v[156:159]
  ds_read_b128 v[192:195], v229 offset:45056
  s_waitcnt lgkmcnt(11)
  v_mfma_f32_16x16x32_bf16 v[160:163], v[204:207], v[196:199], v[160:163]
  ds_read_b128 v[196:199], v229 offset:47104
  v_mfma_f32_16x16x32_bf16 v[164:167], v[204:207], v[234:237], v[164:167]
  s_waitcnt lgkmcnt(8)
  v_mfma_f32_16x16x32_bf16 v[148:151], v[208:211], v[168:171], v[148:151]
  ds_read_b128 v[168:171], v228 offset:49152
  ds_read_b64_tr_b16 v[200:201], v230 offset:16384
  v_mfma_f32_16x16x32_bf16 v[152:155], v[208:211], v[172:175], v[152:155]
  ds_read_b128 v[172:175], v228 offset:51200
  ds_read_b64_tr_b16 v[202:203], v230 offset:16896
  s_waitcnt lgkmcnt(11)
  v_mfma_f32_16x16x32_bf16 v[156:159], v[208:211], v[176:179], v[156:159]
  ds_read_b128 v[176:179], v228 offset:53248
  ds_read_b64_tr_b16 v[204:205], v230 offset:20480
  s_waitcnt lgkmcnt(11)
  v_mfma_f32_16x16x32_bf16 v[160:163], v[208:211], v[180:183], v[160:163]
  ds_read_b128 v[180:183], v228 offset:55296
  ds_read_b64_tr_b16 v[206:207], v230 offset:20992
  v_mfma_f32_16x16x32_bf16 v[164:167], v[208:211], v[234:237], v[164:167]
  s_waitcnt lgkmcnt(11)
  v_mfma_f32_16x16x32_bf16 v[148:151], v[212:215], v[184:187], v[148:151]
  ds_read_b128 v[184:187], v229 offset:49152
  s_waitcnt lgkmcnt(11)
  v_mfma_f32_16x16x32_bf16 v[152:155], v[212:215], v[188:191], v[152:155]
  ds_read_b128 v[188:191], v229 offset:51200
  s_waitcnt lgkmcnt(11)
  v_mfma_f32_16x16x32_bf16 v[156:159], v[212:215], v[192:195], v[156:159]
  ds_read_b128 v[192:195], v229 offset:53248
  s_waitcnt lgkmcnt(11)
  v_mfma_f32_16x16x32_bf16 v[160:163], v[212:215], v[196:199], v[160:163]
  ds_read_b128 v[196:199], v229 offset:55296
  v_mfma_f32_16x16x32_bf16 v[164:167], v[212:215], v[234:237], v[164:167]
  s_waitcnt lgkmcnt(8)
  v_mfma_f32_16x16x32_bf16 v[148:151], v[200:203], v[168:171], v[148:151]
  ds_read_b128 v[168:171], v228 offset:57344
  ds_read_b64_tr_b16 v[208:209], v230 offset:24576
  v_mfma_f32_16x16x32_bf16 v[152:155], v[200:203], v[172:175], v[152:155]
  ds_read_b128 v[172:175], v228 offset:59392
  ds_read_b64_tr_b16 v[210:211], v230 offset:25088
  s_waitcnt lgkmcnt(11)
  v_mfma_f32_16x16x32_bf16 v[156:159], v[200:203], v[176:179], v[156:159]
  ds_read_b128 v[176:179], v228 offset:61440
  ds_read_b64_tr_b16 v[212:213], v230 offset:28672
  s_waitcnt lgkmcnt(11)
  v_mfma_f32_16x16x32_bf16 v[160:163], v[200:203], v[180:183], v[160:163]
  ds_read_b128 v[180:183], v228 offset:63488
  ds_read_b64_tr_b16 v[214:215], v230 offset:29184
  v_mfma_f32_16x16x32_bf16 v[164:167], v[200:203], v[234:237], v[164:167]
  s_waitcnt lgkmcnt(11)
  v_mfma_f32_16x16x32_bf16 v[148:151], v[204:207], v[184:187], v[148:151]
  ds_read_b128 v[184:187], v229 offset:57344
  s_waitcnt lgkmcnt(11)
  v_mfma_f32_16x16x32_bf16 v[152:155], v[204:207], v[188:191], v[152:155]
  ds_read_b128 v[188:191], v229 offset:59392
  s_waitcnt lgkmcnt(11)
  v_mfma_f32_16x16x32_bf16 v[156:159], v[204:207], v[192:195], v[156:159]
  ds_read_b128 v[192:195], v229 offset:61440
  s_waitcnt lgkmcnt(11)
  v_mfma_f32_16x16x32_bf16 v[160:163], v[204:207], v[196:199], v[160:163]
  ds_read_b128 v[196:199], v229 offset:63488
  v_mfma_f32_16x16x32_bf16 v[164:167], v[204:207], v[234:237], v[164:167]
  s_waitcnt lgkmcnt(8)
  v_mfma_f32_16x16x32_bf16 v[164:167], v[208:211], v[234:237], v[164:167]
  ds_read2_b32 v[216:217], v224 offset0:0 offset1:64
  ds_read2_b32 v[218:219], v224 offset0:16 offset1:80
  s_waitcnt lgkmcnt(6)
  v_mfma_f32_16x16x32_bf16 v[164:167], v[212:215], v[234:237], v[164:167]
  ds_read2_b32 v[220:221], v224 offset0:32 offset1:96
  ds_read2_b32 v[222:223], v224 offset0:48 offset1:112
  v_mfma_f32_16x16x32_bf16 v[148:151], v[208:211], v[168:171], v[148:151]
  v_mfma_f32_16x16x32_bf16 v[152:155], v[208:211], v[172:175], v[152:155]
  v_mfma_f32_16x16x32_bf16 v[156:159], v[208:211], v[176:179], v[156:159]
  v_mfma_f32_16x16x32_bf16 v[160:163], v[208:211], v[180:183], v[160:163]
  s_waitcnt lgkmcnt(7)
  v_mfma_f32_16x16x32_bf16 v[148:151], v[212:215], v[184:187], v[148:151]
  s_waitcnt lgkmcnt(6)
  v_mfma_f32_16x16x32_bf16 v[152:155], v[212:215], v[188:191], v[152:155]
  s_waitcnt lgkmcnt(5)
  v_mfma_f32_16x16x32_bf16 v[156:159], v[212:215], v[192:195], v[156:159]
  s_waitcnt lgkmcnt(4)
  v_mfma_f32_16x16x32_bf16 v[160:163], v[212:215], v[196:199], v[160:163]
  s_mov_b32 s55, 0x3eb8aa3b
  v_mov_b32_dpp v164, v164 quad_perm:[0,0,0,0] row_mask:0xf bank_mask:0xf
  v_mov_b32_dpp v165, v165 quad_perm:[0,0,0,0] row_mask:0xf bank_mask:0xf
  v_mov_b32_dpp v166, v166 quad_perm:[0,0,0,0] row_mask:0xf bank_mask:0xf
  v_mov_b32_dpp v167, v167 quad_perm:[0,0,0,0] row_mask:0xf bank_mask:0xf
  s_waitcnt lgkmcnt(3)
  v_mul_f32 v216, s55, v216
  s_waitcnt lgkmcnt(2)
  v_mul_f32 v218, s55, v218
  s_waitcnt lgkmcnt(1)
  v_mul_f32 v220, s55, v220
  s_waitcnt lgkmcnt(0)
  v_mul_f32 v222, s55, v222
  v_pk_fma_f32 v[148:149], v[216:217], v[164:165], v[148:149] op_sel:[1,0,0] op_sel_hi:[1,1,1]
  v_pk_fma_f32 v[150:151], v[216:217], v[166:167], v[150:151] op_sel:[1,0,0] op_sel_hi:[1,1,1]
  v_pk_mul_f32 v[148:149], v[148:149], v[216:217] op_sel_hi:[1,0]
  v_pk_mul_f32 v[150:151], v[150:151], v[216:217] op_sel_hi:[1,0]
  v_cvt_pk_bf16_f32 v44, v148, v149
  v_cvt_pk_bf16_f32 v45, v150, v151
  v_pk_fma_f32 v[152:153], v[218:219], v[164:165], v[152:153] op_sel:[1,0,0] op_sel_hi:[1,1,1]
  v_pk_fma_f32 v[154:155], v[218:219], v[166:167], v[154:155] op_sel:[1,0,0] op_sel_hi:[1,1,1]
  v_pk_mul_f32 v[152:153], v[152:153], v[218:219] op_sel_hi:[1,0]
  v_pk_mul_f32 v[154:155], v[154:155], v[218:219] op_sel_hi:[1,0]
  v_cvt_pk_bf16_f32 v46, v152, v153
  v_cvt_pk_bf16_f32 v47, v154, v155
  v_pk_fma_f32 v[156:157], v[220:221], v[164:165], v[156:157] op_sel:[1,0,0] op_sel_hi:[1,1,1]
  v_pk_fma_f32 v[158:159], v[220:221], v[166:167], v[158:159] op_sel:[1,0,0] op_sel_hi:[1,1,1]
  v_pk_mul_f32 v[156:157], v[156:157], v[220:221] op_sel_hi:[1,0]
  v_pk_mul_f32 v[158:159], v[158:159], v[220:221] op_sel_hi:[1,0]
  v_cvt_pk_bf16_f32 v40, v156, v157
  v_cvt_pk_bf16_f32 v41, v158, v159
  v_pk_fma_f32 v[160:161], v[222:223], v[164:165], v[160:161] op_sel:[1,0,0] op_sel_hi:[1,1,1]
  v_pk_fma_f32 v[162:163], v[222:223], v[166:167], v[162:163] op_sel:[1,0,0] op_sel_hi:[1,1,1]
  v_pk_mul_f32 v[160:161], v[160:161], v[222:223] op_sel_hi:[1,0]
  v_pk_mul_f32 v[162:163], v[162:163], v[222:223] op_sel_hi:[1,0]
  v_cvt_pk_bf16_f32 v42, v160, v161
  v_cvt_pk_bf16_f32 v43, v162, v163
  v_permlane16_swap_b32 v44, v46
  v_permlane16_swap_b32 v45, v47
  v_permlane16_swap_b32 v40, v42
  v_permlane16_swap_b32 v41, v43
  ds_write_b128 v225, v[44:47]
  ds_write_b128 v225, v[40:43] offset:1024
  s_waitcnt lgkmcnt(0)
  s_barrier
  s_cmp_lg_u32 s70, 0
  s_cselect_b32 s42, s38, 0
  buffer_load_dwordx4 v[88:91], v2, s[40:43], s70 offen sc1
  s_mov_b64 exec, s[66:67]
  buffer_load_dwordx4 v[64:67], v4, s[40:43], s70 offen offset:1024 sc1
  buffer_load_dwordx4 v[68:71], v4, s[40:43], s70 offen offset:1536 sc1
  s_mov_b64 exec, -1
  buffer_load_dword v28, v3, s[40:43], s70 offen offset:2048 sc1
  s_cmp_lg_u32 s71, 0
  s_cselect_b32 s42, s38, 0
  buffer_load_dwordx4 v[92:95], v2, s[40:43], s71 offen sc1
  s_mov_b64 exec, s[66:67]
  buffer_load_dwordx4 v[72:75], v4, s[40:43], s71 offen offset:1024 sc1
  buffer_load_dwordx4 v[76:79], v4, s[40:43], s71 offen offset:1536 sc1
  s_mov_b64 exec, -1
  buffer_load_dword v29, v3, s[40:43], s71 offen offset:2048 sc1
  s_cmp_lg_u32 s72, 0
  s_cselect_b32 s42, s38, 0
  buffer_load_dwordx4 v[96:99], v2, s[40:43], s72 offen sc1
  s_mov_b64 exec, s[66:67]
  buffer_load_dwordx4 v[80:83], v4, s[40:43], s72 offen offset:1024 sc1
  buffer_load_dwordx4 v[84:87], v4, s[40:43], s72 offen offset:1536 sc1
  s_mov_b64 exec, -1
  buffer_load_dword v30, v3, s[40:43], s72 offen offset:2048 sc1
  s_cmp_lg_u32 s73, 0
  s_cselect_b32 s42, s38, 0
  buffer_load_dwordx4 v[36:39], v2, s[40:43], s73 offen sc1
  s_mov_b64 exec, s[66:67]
  buffer_load_dwordx4 v[56:59], v4, s[40:43], s73 offen offset:1024 sc1
  buffer_load_dwordx4 v[60:63], v4, s[40:43], s73 offen offset:1536 sc1
  s_mov_b64 exec, -1
  buffer_load_dword v31, v3, s[40:43], s73 offen offset:2048 sc1
; own key tile (tile 1) from the K/V wave
  ds_read_b128 v[32:35], v226 offset:0
  s_mov_b64 exec, s[66:67]
  ds_read_b128 v[48:51], v227 offset:2048
  ds_read_b128 v[52:55], v227 offset:2560
  s_mov_b64 exec, -1
  v_mov_b64 v[172:173], 0
  v_mov_b64 v[174:175], 0
  v_mov_b64 v[176:177], 0
  v_mov_b64 v[178:179], 0
  v_mov_b64 v[180:181], 0
  v_mov_b64 v[182:183], 0
  v_mov_b64 v[184:185], 0
  v_mov_b64 v[186:187], 0
  s_waitcnt lgkmcnt(0)
  s_setprio 1
  s_branch .Lfb_att
.Lfb_kv:
; ==== waves 0-3: K and V projections of head hd for the 64 tokens, LN1 statistics
  v_lshlrev_b32 v2, 4, v1
  v_lshlrev_b32 v3, 2, v1
  v_and_b32 v36, 32, v1
  v_and_b32 v4, 0xf0, v2
  v_lshl_or_b32 v4, v36, 3, v4
  s_mov_b32 s19, 0x20000
  s_cmp_eq_u32 s44, 0
  s_cselect_b32 s18, 1024, 0
  s_cbranch_scc0 .Lfb_nowr
  s_mov_b32 m0, 160256
  s_waitcnt lgkmcnt(0)
  buffer_load_dwordx4 v2, s[16:19], 0 offen lds
.Lfb_nowr:
  s_waitcnt lgkmcnt(0)
; wave 1 pulls a few lines of this kernel's code into the L2 ahead of the instruction fetch
  s_and_b32 s16, s64, 0xffffff80
  s_mov_b32 s17, s65
  s_cmp_eq_u32 s44, 1
  s_cselect_b32 s18, .Lfb_end-.Lfb_begin, 0
  s_lshr_b32 s55, s2, 3
  v_lshl_add_u32 v36, v1, 5, s55
  v_lshlrev_b32 v36, 7, v36
  buffer_load_dword v36, v36, s[16:19], 0 offen
  s_load_dword s51, s[36:37], 0
  s_load_dwordx2 s[12:13], s[0:1], 16
  s_load_dwordx2 s[20:21], s[0:1], 32
  s_load_dwordx2 s[24:25], s[0:1], 40
  s_load_dwordx2 s[28:29], s[0:1], 48
  s_load_dwordx2 s[32:33], s[0:1], 56
  s_mov_b32 s38, 7995392
  s_mov_b32 s14, 524288
  s_mov_b32 s34, 2097152
  s_cmp_lt_u32 s47, 28
  s_cselect_b32 s22, 1835008, 0
  s_mov_b32 s26, s22
  s_mov_b32 s30, s22
  s_mov_b32 s39, 0x20000
  s_mov_b32 s15, 0x20000
  s_mov_b32 s35, 0x20000
  s_mov_b32 s23, 0x20000
  s_mov_b32 s27, 0x20000
  s_mov_b32 s31, 0x20000
  s_mov_b32 s43, 0x20000
  s_mov_b64 s[40:41], s[36:37]
  s_mov_b32 s66, 0xffff
  s_mov_b32 s67, 0xffff
  s_mov_b32 s54, 0x40000
  s_lshl_b32 s63, s44, 10
  s_lshl2_add_u32 s59, s48, s45
  s_lshl_b32 s60, s47, 5
  s_add_u32 s60, s60, s59
  s_lshl1_add_u32 s60, s60, s46
  s_mul_i32 s60, s60, 2304
  s_add_u32 s53, s60, 4096
  s_lshl1_add_u32 s60, s47, s46
  s_lshl_b32 s60, s60, 6
  s_add_u32 s60, s60, s59
  s_lshl_b32 s60, s60, 9
  s_add_u32 s62, s60, 5242880
  s_and_b32 s59, s47, 7
  s_and_b32 s60, s48, 3
  s_xor_b32 s60, s60, s59
  s_lshl_b32 s75, s60, 16
  s_lshl_b32 s59, s59, 13
  s_or_b32 s74, s59, s63
  v_lshrrev_b32 v37, 3, v1
  v_and_b32 v38, 31, v1
  v_and_b32 v37, 4, v37
  v_sub_u32 v16, v38, v37
  v_lshlrev_b32_e64 v38, v38, -2
  v_not_b32 v38, v38
  v_lshrrev_b32 v38, v37, v38
  v_bfe_i32 v100, v38, 0, 1
  v_bfe_i32 v101, v38, 1, 1
  v_bfe_i32 v102, v38, 2, 1
  v_bfe_i32 v103, v38, 3, 1
  v_bfe_i32 v104, v38, 8, 1
  v_bfe_i32 v105, v38, 9, 1
  v_bfe_i32 v106, v38, 10, 1
  v_bfe_i32 v107, v38, 11, 1
  v_bfe_i32 v108, v38, 16, 1
  v_bfe_i32 v109, v38, 17, 1
  v_bfe_i32 v110, v38, 18, 1
  v_bfe_i32 v111, v38, 19, 1
  v_bfe_i32 v112, v38, 24, 1
  v_bfe_i32 v113, v38, 25, 1
  v_bfe_i32 v114, v38, 26, 1
  v_bfe_i32 v115, v38, 27, 1
  v_bfi_b32 v116, s66, v100, v101
  v_bfi_b32 v117, s66, v102, v103
  v_bfi_b32 v118, s66, v104, v105
  v_bfi_b32 v119, s66, v106, v107
  v_bfi_b32 v120, s66, v108, v109
  v_bfi_b32 v121, s66, v110, v111
  v_bfi_b32 v122, s66, v112, v113
  v_bfi_b32 v123, s66, v114, v115
  v_lshrrev_b32 v37, 3, v1
  v_and_b32 v38, 7, v1
  v_bitop3_b32 v39, v38, v37, 6 bitop3:0x78
  s_lshl_b32 s55, s44, 13
  s_lshl_b32 s56, s48, 7
  s_add_u32 s55, s55, s56
  v_lshl_add_u32 v5, v37, 10, s55
  v_lshl_add_u32 v5, v39, 4, v5
  v_bitop3_b32 v39, v38, v37, 2 bitop3:0x78
  s_and_b32 s57, s44, 1
  s_lshl_b32 s57, s57, 2
  v_xor_b32 v39, s57, v39
  s_lshl_b32 s57, s47, 16
  s_add_u32 s55, s55, s57
  v_lshl_add_u32 v15, v37, 10, s55
  v_lshl_add_u32 v15, v39, 4, v15
  s_lshl3_add_u32 s55, s48, s44
  s_mul_i32 s55, s55, 28672
  s_lshl_b32 s57, s47, 7
  s_add_u32 s55, s55, s57
  v_mul_u32_u24 v14, 3584, v37
  v_lshl_add_u32 v14, v39, 4, v14
  v_add_u32 v14, s55, v14
  s_mul_i32 s55, s45, 2304
  s_add_u32 s56, s55, 65536
  s_add_u32 s55, s55, 74752
  v_add_u32 v6, s56, v2
  v_add_u32 v8, s56, v3
  v_add_u32 v7, s55, v2
  v_add_u32 v9, s55, v3
  v_and_b32 v37, 16, v1
  v_lshl_or_b32 v10, v37, 10, v4
  v_and_b32 v38, 32, v1
  v_lshl_or_b32 v38, v37, 3, v38
  v_and_b32 v39, 14, v1
  v_lshl_or_b32 v38, v39, 1, v38
  s_lshr_b32 s55, s45, 1
  s_lshl_b32 s55, s55, 9
  s_and_b32 s56, s45, 1
  s_lshl_b32 s56, s56, 6
  s_add_u32 s55, s55, s56
  s_add_u32 s55, s55, 161280
  v_add_u32 v11, s55, v38
  v_xor_b32 v12, 0, v2
  v_lshrrev_b32 v13, 3, v12
  v_and_b32 v13, 0x7c, v13
  v_bfe_u32 v37, v1, 2, 3
  v_and_b32 v38, 32, v1
  v_lshlrev_b32 v38, 6, v38
  v_lshl_or_b32 v38, v37, 7, v38
  v_and_b32 v37, 3, v1
  v_lshl_or_b32 v38, v37, 3, v38
  v_and_b32 v37, 24, v1
  v_lshl_or_b32 v38, v37, 2, v38
  v_xor_b32 v24, 0, v38
  v_xor_b32 v25, 32, v38
  v_xor_b32 v26, 64, v38
  v_xor_b32 v27, 96, v38
  s_sub_u32 s55, s48, s49
  s_and_b32 s55, s55, 7
  s_lshl_b32 s55, s55, 13
  s_add_u32 s55, s55, s52
  s_add_u32 s55, s55, 0
  v_lshrrev_b32 v37, 3, v1
  v_xor_b32 v37, 0, v37
  v_and_b32 v38, 48, v1
  v_lshlrev_b32 v38, 5, v38
  v_lshl_or_b32 v38, v37, 4, v38
  v_and_b32 v37, 7, v1
  v_lshl_or_b32 v38, v37, 1, v38
  v_add_u32 v17, s55, v38
  s_add_u32 s55, s52, 106496
  v_lshl_add_u32 v18, v1, 3, s55
  v_add_u32 v19, 8192, v18
  s_lshl_b32 s55, s48, 7
  v_lshl_add_u32 v20, v1, 1, s55
  v_and_b32 v37, 15, v1
  s_add_u32 s55, s55, 160256
  v_lshl_add_u32 v21, v37, 1, s55
  v_bfe_u32 v41, v1, 1, 3
  v_lshrrev_b32 v42, 4, v1
  v_xor_b32 v41, v41, v42
  v_and_b32 v37, 15, v1
  v_lshlrev_b32 v37, 7, v37
  v_lshl_or_b32 v37, v41, 4, v37
  v_xor_b32 v38, 64, v37
  v_bfe_u32 v41, v1, 2, 2
  v_lshl_or_b32 v41, v42, 3, v41
  v_and_b32 v42, 3, v1
  v_cmp_eq_u32 vcc, 0, v42
  v_lshlrev_b32 v42, 3, v42
  v_lshl_or_b32 v39, v41, 7, v42
  v_mov_b32 v42, 1065369472
  v_bfe_u32 v41, v1, 3, 2
  v_cndmask_b32_e64 v44, 0, v42, vcc
  v_xor_b32 v41, s45, v41
  v_mov_b32 v45, v44
  v_lshl_or_b32 v39, v41, 5, v39
  v_mov_b64 v[46:47], v[44:45]
  v_add_u32 v40, 106496, v39
  v_add_u32 v39, 65536, v39
  v_add_u32 v164, s52, v37
  v_add_u32 v165, s52, v38
  v_and_b32 v167, 60, v3
  v_add_u32 v167, 159744, v167
  v_and_b32 v168, 48, v1
  v_add_u32 v168, 159744, v168
  v_lshrrev_b32 v41, 2, v1
  v_bitop3_b32 v41, v41, v1, 12 bitop3:0x28
  s_lshl_b32 s55, s45, 6
  v_cmp_eq_u32 vcc, 0, v41
  v_add_u32 v166, s55, v167
  v_mov_b32 v41, 162560
  v_cndmask_b32_e64 v166, v41, v166, vcc
  s_mul_i32 s55, s45, 3072
  s_add_u32 s55, s55, 147456
  v_add_u32 v169, s55, v2
  v_add_u32 v170, s55, v4
  v_and_b32 v22, 27, v1
  v_mov_b32 v23, 1065369472
  v_cmp_eq_u32 vcc, 16, v22
  s_nop 1
  v_cndmask_b32_e64 v22, 0, v23, vcc
  v_mov_b32 v23, v22
  v_mov_b64 v[48:49], v[22:23]
  v_mov_b64 v[50:51], v[22:23]
  v_mov_b64 v[52:53], v[22:23]
  v_mov_b64 v[54:55], v[22:23]
  v_mov_b64 v[140:141], v[22:23]
  v_mov_b64 v[142:143], v[22:23]
  v_mov_b64 v[144:145], v[22:23]
  v_mov_b64 v[146:147], v[22:23]
  s_mul_i32 s59, s47, 43
  s_lshr_b32 s59, s59, 7
  s_mul_i32 s59, s59, 3
  s_sub_u32 s59, s47, s59
  s_cmp_eq_u32 s59, 0
  s_cselect_b32 s59, 3, s59
  s_mul_i32 s60, s59, 147456
  s_sub_u32 s60, s53, s60
  s_cmp_ge_u32 s47, s59
  s_cselect_b32 s70, s60, 0
  s_sub_u32 s59, s59, 1
  s_cmp_eq_u32 s59, 0
  s_cselect_b32 s59, 3, s59
  s_mul_i32 s60, s59, 147456
  s_sub_u32 s60, s53, s60
  s_cmp_ge_u32 s47, s59
  s_cselect_b32 s71, s60, 0
  s_sub_u32 s59, s59, 1
  s_cmp_eq_u32 s59, 0
  s_cselect_b32 s59, 3, s59
  s_mul_i32 s60, s59, 147456
  s_sub_u32 s60, s53, s60
  s_cmp_ge_u32 s47, s59
  s_cselect_b32 s72, s60, 0
  s_mov_b32 s59, 4
  s_mul_i32 s60, s59, 147456
  s_sub_u32 s60, s53, s60
  s_cmp_ge_u32 s47, s59
  s_cselect_b32 s73, s60, 0
  s_mov_b32 s68, 0xaaaaaaaa
  s_mov_b32 s69, 0xaaaaaaaa
  s_mov_b32 s64, 0xcccccccc
  s_mov_b32 s65, 0xcccccccc
  v_mov_b32 v41, 162304
  v_mov_b32 v42, 0
  ds_write_b32 v41, v42
  s_waitcnt lgkmcnt(0)
  s_add_u32 s51, s51, 1
  s_mul_i32 s50, s51, 0x9E3779B1
  s_and_b32 s61, s51, 0xff
  s_or_b32 s50, s50, 1
  s_waitcnt vmcnt(0) lgkmcnt(0)
  s_barrier
  ds_read_b64_tr_b16 v[200:201], v39 offset:0
  ds_read_b64_tr_b16 v[202:203], v39 offset:512
  ds_read_b64_tr_b16 v[204:205], v39 offset:8192
  ds_read_b64_tr_b16 v[206:207], v39 offset:8704
  ds_read_b128 v[128:131], v37 offset:0
  ds_read_b128 v[132:135], v37 offset:2048
  ds_read_b128 v[136:139], v37 offset:4096
  ds_read_b128 v[172:175], v37 offset:6144
  ds_read_b64_tr_b16 v[208:209], v39 offset:4096
  ds_read_b64_tr_b16 v[210:211], v39 offset:4608
  ds_read_b64_tr_b16 v[212:213], v39 offset:12288
  ds_read_b64_tr_b16 v[214:215], v39 offset:12800
  ds_read_b128 v[176:179], v38 offset:0
  ds_read_b128 v[180:183], v38 offset:2048
  ds_read_b128 v[184:187], v38 offset:4096
  ds_read_b128 v[188:191], v38 offset:6144
  ds_read_b128 v[156:159], v164 offset:0
  ds_read_b128 v[160:163], v165 offset:0
  s_waitcnt lgkmcnt(14)
  v_mfma_f32_16x16x32_bf16 v[88:91], v[200:203], v[44:47], 0
  ds_read_b128 v[148:151], v164 offset:8192
  v_mfma_f32_16x16x32_bf16 v[92:95], v[44:47], v[204:207], 0
  ds_read_b128 v[152:155], v165 offset:8192
  s_waitcnt lgkmcnt(14)
  v_mfma_f32_16x16x32_bf16 v[56:59], v[200:203], v[128:131], 0
  ds_read_b128 v[192:195], v38 offset:12288
  v_mfma_f32_16x16x32_bf16 v[72:75], v[128:131], v[204:207], 0
  ds_read_b128 v[128:131], v37 offset:8192
  v_mfma_f32_16x16x32_bf16 v[60:63], v[200:203], v[132:135], 0
  ds_read_b128 v[196:199], v38 offset:14336
  v_mfma_f32_16x16x32_bf16 v[76:79], v[132:135], v[204:207], 0
  ds_read_b128 v[132:135], v37 offset:10240
  s_waitcnt lgkmcnt(14)
  v_mfma_f32_16x16x32_bf16 v[64:67], v[200:203], v[136:139], 0
  v_mfma_f32_16x16x32_bf16 v[80:83], v[136:139], v[204:207], 0
  ds_read_b128 v[136:139], v37 offset:12288
  v_mfma_f32_16x16x32_bf16 v[68:71], v[200:203], v[172:175], 0
  v_mfma_f32_16x16x32_bf16 v[84:87], v[172:175], v[204:207], 0
  ds_read_b128 v[172:175], v37 offset:14336
  s_waitcnt lgkmcnt(7)
  v_mfma_f32_16x16x32_bf16 v[96:99], v[148:151], v[148:151], 0
  v_mfma_f32_16x16x32_bf16 v[124:127], v[44:47], v[148:151], 0
  s_waitcnt lgkmcnt(6)
  v_mfma_f32_16x16x32_bf16 v[96:99], v[152:155], v[152:155], v[96:99]
  v_mfma_f32_16x16x32_bf16 v[124:127], v[44:47], v[152:155], v[124:127]
  v_mfma_f32_16x16x32_bf16 v[96:99], v[156:159], v[156:159], v[96:99]
  v_mfma_f32_16x16x32_bf16 v[124:127], v[44:47], v[156:159], v[124:127]
  v_mfma_f32_16x16x32_bf16 v[96:99], v[160:163], v[160:163], v[96:99]
  v_mfma_f32_16x16x32_bf16 v[124:127], v[44:47], v[160:163], v[124:127]
  v_mfma_f32_16x16x32_bf16 v[56:59], v[208:211], v[176:179], v[56:59]
  v_mfma_f32_16x16x32_bf16 v[72:75], v[176:179], v[212:215], v[72:75]
  ds_read_b128 v[176:179], v38 offset:8192
  v_mfma_f32_16x16x32_bf16 v[60:63], v[208:211], v[180:183], v[60:63]
  v_mfma_f32_16x16x32_bf16 v[76:79], v[180:183], v[212:215], v[76:79]
  ds_read_b128 v[180:183], v38 offset:10240
  s_barrier
  v_mfma_f32_16x16x32_bf16 v[64:67], v[208:211], v[184:187], v[64:67]
  ds_read_b64_tr_b16 v[200:201], v39 offset:16384
  ds_read_b64_tr_b16 v[202:203], v39 offset:16896
  v_mfma_f32_16x16x32_bf16 v[80:83], v[184:187], v[212:215], v[80:83]
  ds_read_b64_tr_b16 v[204:205], v39 offset:24576
  ds_read_b64_tr_b16 v[206:207], v39 offset:25088
  v_mfma_f32_16x16x32_bf16 v[68:71], v[208:211], v[188:191], v[68:71]
  ds_read_b64_tr_b16 v[216:217], v39 offset:20480
  ds_read_b64_tr_b16 v[218:219], v39 offset:20992
  v_mfma_f32_16x16x32_bf16 v[84:87], v[188:191], v[212:215], v[84:87]
  ds_read_b64_tr_b16 v[220:221], v39 offset:28672
  ds_read_b64_tr_b16 v[222:223], v39 offset:29184
  v_mfma_f32_16x16x32_bf16 v[88:91], v[208:211], v[44:47], v[88:91]
  v_mfma_f32_16x16x32_bf16 v[92:95], v[44:47], v[212:215], v[92:95]
  s_waitcnt lgkmcnt(6)
  v_mfma_f32_16x16x32_bf16 v[88:91], v[200:203], v[44:47], v[88:91]
  ds_read_b128 v[148:151], v164 offset:16384
  s_waitcnt lgkmcnt(5)
  v_mfma_f32_16x16x32_bf16 v[92:95], v[44:47], v[204:207], v[92:95]
  ds_read_b128 v[152:155], v165 offset:16384
  v_mfma_f32_16x16x32_bf16 v[56:59], v[200:203], v[128:131], v[56:59]
  ds_read_b128 v[184:187], v38 offset:20480
  v_mfma_f32_16x16x32_bf16 v[72:75], v[128:131], v[204:207], v[72:75]
  ds_read_b128 v[128:131], v37 offset:16384
  v_mfma_f32_16x16x32_bf16 v[60:63], v[200:203], v[132:135], v[60:63]
  ds_read_b128 v[188:191], v38 offset:22528
  v_mfma_f32_16x16x32_bf16 v[76:79], v[132:135], v[204:207], v[76:79]
  ds_read_b128 v[132:135], v37 offset:18432
  v_mfma_f32_16x16x32_bf16 v[64:67], v[200:203], v[136:139], v[64:67]
  v_mfma_f32_16x16x32_bf16 v[80:83], v[136:139], v[204:207], v[80:83]
  ds_read_b128 v[136:139], v37 offset:20480
  v_mfma_f32_16x16x32_bf16 v[68:71], v[200:203], v[172:175], v[68:71]
  v_mfma_f32_16x16x32_bf16 v[84:87], v[172:175], v[204:207], v[84:87]
  ds_read_b128 v[172:175], v37 offset:22528
  s_waitcnt lgkmcnt(7)
  v_mfma_f32_16x16x32_bf16 v[96:99], v[148:151], v[148:151], v[96:99]
  v_mfma_f32_16x16x32_bf16 v[124:127], v[44:47], v[148:151], v[124:127]
  s_waitcnt lgkmcnt(6)
  v_mfma_f32_16x16x32_bf16 v[96:99], v[152:155], v[152:155], v[96:99]
  v_mfma_f32_16x16x32_bf16 v[124:127], v[44:47], v[152:155], v[124:127]
  v_mfma_f32_16x16x32_bf16 v[56:59], v[216:219], v[176:179], v[56:59]
  v_mfma_f32_16x16x32_bf16 v[72:75], v[176:179], v[220:223], v[72:75]
  ds_read_b128 v[176:179], v38 offset:16384
  v_mfma_f32_16x16x32_bf16 v[60:63], v[216:219], v[180:183], v[60:63]
  v_mfma_f32_16x16x32_bf16 v[76:79], v[180:183], v[220:223], v[76:79]
  ds_read_b128 v[180:183], v38 offset:18432
  s_barrier
  v_mfma_f32_16x16x32_bf16 v[64:67], v[216:219], v[192:195], v[64:67]
  ds_read_b64_tr_b16 v[200:201], v39 offset:32768
  ds_read_b64_tr_b16 v[202:203], v39 offset:33280
  v_mfma_f32_16x16x32_bf16 v[80:83], v[192:195], v[220:223], v[80:83]
  ds_read_b64_tr_b16 v[204:205], v40 offset:0
  ds_read_b64_tr_b16 v[206:207], v40 offset:512
  v_mfma_f32_16x16x32_bf16 v[68:71], v[216:219], v[196:199], v[68:71]
  ds_read_b64_tr_b16 v[208:209], v39 offset:36864
  ds_read_b64_tr_b16 v[210:211], v39 offset:37376
  v_mfma_f32_16x16x32_bf16 v[84:87], v[196:199], v[220:223], v[84:87]
  ds_read_b64_tr_b16 v[212:213], v40 offset:4096
  ds_read_b64_tr_b16 v[214:215], v40 offset:4608
  v_mfma_f32_16x16x32_bf16 v[88:91], v[216:219], v[44:47], v[88:91]
  v_mfma_f32_16x16x32_bf16 v[92:95], v[44:47], v[220:223], v[92:95]
  s_waitcnt lgkmcnt(6)
  v_mfma_f32_16x16x32_bf16 v[88:91], v[200:203], v[44:47], v[88:91]
  ds_read_b128 v[148:151], v164 offset:24576
  s_waitcnt lgkmcnt(5)
  v_mfma_f32_16x16x32_bf16 v[92:95], v[44:47], v[204:207], v[92:95]
  ds_read_b128 v[152:155], v165 offset:24576
  v_mfma_f32_16x16x32_bf16 v[56:59], v[200:203], v[128:131], v[56:59]
  ds_read_b128 v[192:195], v38 offset:28672
  v_mfma_f32_16x16x32_bf16 v[72:75], v[128:131], v[204:207], v[72:75]
  ds_read_b128 v[128:131], v37 offset:24576
  v_mfma_f32_16x16x32_bf16 v[60:63], v[200:203], v[132:135], v[60:63]
  ds_read_b128 v[196:199], v38 offset:30720
  v_mfma_f32_16x16x32_bf16 v[76:79], v[132:135], v[204:207], v[76:79]
  ds_read_b128 v[132:135], v37 offset:26624
  v_mfma_f32_16x16x32_bf16 v[64:67], v[200:203], v[136:139], v[64:67]
  v_mfma_f32_16x16x32_bf16 v[80:83], v[136:139], v[204:207], v[80:83]
  ds_read_b128 v[136:139], v37 offset:28672
  v_mfma_f32_16x16x32_bf16 v[68:71], v[200:203], v[172:175], v[68:71]
  v_mfma_f32_16x16x32_bf16 v[84:87], v[172:175], v[204:207], v[84:87]
  ds_read_b128 v[172:175], v37 offset:30720
  s_waitcnt lgkmcnt(7)
  v_mfma_f32_16x16x32_bf16 v[96:99], v[148:151], v[148:151], v[96:99]
  v_mfma_f32_16x16x32_bf16 v[124:127], v[44:47], v[148:151], v[124:127]
  s_waitcnt lgkmcnt(6)
  v_mfma_f32_16x16x32_bf16 v[96:99], v[152:155], v[152:155], v[96:99]
  v_mfma_f32_16x16x32_bf16 v[124:127], v[44:47], v[152:155], v[124:127]
  v_mfma_f32_16x16x32_bf16 v[56:59], v[208:211], v[176:179], v[56:59]
  v_mfma_f32_16x16x32_bf16 v[72:75], v[176:179], v[212:215], v[72:75]
  ds_read_b128 v[176:179], v38 offset:24576
  v_mfma_f32_16x16x32_bf16 v[60:63], v[208:211], v[180:183], v[60:63]
  v_mfma_f32_16x16x32_bf16 v[76:79], v[180:183], v[212:215], v[76:79]
  ds_read_b128 v[180:183], v38 offset:26624
  s_barrier
  v_mfma_f32_16x16x32_bf16 v[64:67], v[208:211], v[184:187], v[64:67]
  ds_read_b64_tr_b16 v[200:201], v40 offset:8192
  ds_read_b64_tr_b16 v[202:203], v40 offset:8704
  v_mfma_f32_16x16x32_bf16 v[80:83], v[184:187], v[212:215], v[80:83]
  ds_read_b64_tr_b16 v[204:205], v40 offset:16384
  ds_read_b64_tr_b16 v[206:207], v40 offset:16896
  v_mfma_f32_16x16x32_bf16 v[68:71], v[208:211], v[188:191], v[68:71]
  ds_read_b64_tr_b16 v[216:217], v40 offset:12288
  ds_read_b64_tr_b16 v[218:219], v40 offset:12800
  v_mfma_f32_16x16x32_bf16 v[84:87], v[188:191], v[212:215], v[84:87]
  ds_read_b64_tr_b16 v[220:221], v40 offset:20480
  ds_read_b64_tr_b16 v[222:223], v40 offset:20992
  v_mfma_f32_16x16x32_bf16 v[88:91], v[208:211], v[44:47], v[88:91]
  v_mfma_f32_16x16x32_bf16 v[92:95], v[44:47], v[212:215], v[92:95]
  s_waitcnt lgkmcnt(6)
  v_mfma_f32_16x16x32_bf16 v[88:91], v[200:203], v[44:47], v[88:91]
  ds_read_b128 v[148:151], v164 offset:32768
  s_waitcnt lgkmcnt(5)
  v_mfma_f32_16x16x32_bf16 v[92:95], v[44:47], v[204:207], v[92:95]
  ds_read_b128 v[152:155], v165 offset:32768
  v_mfma_f32_16x16x32_bf16 v[56:59], v[200:203], v[128:131], v[56:59]
  ds_read_b128 v[184:187], v38 offset:36864
  v_mfma_f32_16x16x32_bf16 v[72:75], v[128:131], v[204:207], v[72:75]
  ds_read_b128 v[128:131], v37 offset:32768
  v_mfma_f32_16x16x32_bf16 v[60:63], v[200:203], v[132:135], v[60:63]
  ds_read_b128 v[188:191], v38 offset:38912
  v_mfma_f32_16x16x32_bf16 v[76:79], v[132:135], v[204:207], v[76:79]
  ds_read_b128 v[132:135], v37 offset:34816
  v_mfma_f32_16x16x32_bf16 v[64:67], v[200:203], v[136:139], v[64:67]
  v_mfma_f32_16x16x32_bf16 v[80:83], v[136:139], v[204:207], v[80:83]
  ds_read_b128 v[136:139], v37 offset:36864
  v_mfma_f32_16x16x32_bf16 v[68:71], v[200:203], v[172:175], v[68:71]
  v_mfma_f32_16x16x32_bf16 v[84:87], v[172:175], v[204:207], v[84:87]
  ds_read_b128 v[172:175], v37 offset:38912
  s_waitcnt lgkmcnt(7)
  v_mfma_f32_16x16x32_bf16 v[96:99], v[148:151], v[148:151], v[96:99]
  v_mfma_f32_16x16x32_bf16 v[124:127], v[44:47], v[148:151], v[124:127]
  s_waitcnt lgkmcnt(6)
  v_mfma_f32_16x16x32_bf16 v[96:99], v[152:155], v[152:155], v[96:99]
  v_mfma_f32_16x16x32_bf16 v[124:127], v[44:47], v[152:155], v[124:127]
  v_mfma_f32_16x16x32_bf16 v[56:59], v[216:219], v[176:179], v[56:59]
  v_mfma_f32_16x16x32_bf16 v[72:75], v[176:179], v[220:223], v[72:75]
  ds_read_b128 v[176:179], v38 offset:32768
  v_mfma_f32_16x16x32_bf16 v[60:63], v[216:219], v[180:183], v[60:63]
  v_mfma_f32_16x16x32_bf16 v[76:79], v[180:183], v[220:223], v[76:79]
  ds_read_b128 v[180:183], v38 offset:34816
  s_barrier
  v_mfma_f32_16x16x32_bf16 v[64:67], v[216:219], v[192:195], v[64:67]
  ds_read_b64_tr_b16 v[200:201], v40 offset:24576
  ds_read_b64_tr_b16 v[202:203], v40 offset:25088
  v_mfma_f32_16x16x32_bf16 v[80:83], v[192:195], v[220:223], v[80:83]
  ds_read_b64_tr_b16 v[204:205], v40 offset:32768
  ds_read_b64_tr_b16 v[206:207], v40 offset:33280
  v_mfma_f32_16x16x32_bf16 v[68:71], v[216:219], v[196:199], v[68:71]
  ds_read_b64_tr_b16 v[208:209], v40 offset:28672
  ds_read_b64_tr_b16 v[210:211], v40 offset:29184
  v_mfma_f32_16x16x32_bf16 v[84:87], v[196:199], v[220:223], v[84:87]
  ds_read_b64_tr_b16 v[212:213], v40 offset:36864
  ds_read_b64_tr_b16 v[214:215], v40 offset:37376
  v_mfma_f32_16x16x32_bf16 v[88:91], v[216:219], v[44:47], v[88:91]
  v_mfma_f32_16x16x32_bf16 v[92:95], v[44:47], v[220:223], v[92:95]
  s_waitcnt lgkmcnt(6)
  v_mfma_f32_16x16x32_bf16 v[88:91], v[200:203], v[44:47], v[88:91]
  ds_read_b128 v[148:151], v164 offset:40960
  s_waitcnt lgkmcnt(5)
  v_mfma_f32_16x16x32_bf16 v[92:95], v[44:47], v[204:207], v[92:95]
  ds_read_b128 v[152:155], v165 offset:40960
  v_mfma_f32_16x16x32_bf16 v[56:59], v[200:203], v[128:131], v[56:59]
  ds_read_b128 v[192:195], v38 offset:45056
  v_mfma_f32_16x16x32_bf16 v[72:75], v[128:131], v[204:207], v[72:75]
  ds_read_b128 v[128:131], v37 offset:40960
  v_mfma_f32_16x16x32_bf16 v[60:63], v[200:203], v[132:135], v[60:63]
  ds_read_b128 v[196:199], v38 offset:47104
  v_mfma_f32_16x16x32_bf16 v[76:79], v[132:135], v[204:207], v[76:79]
  ds_read_b128 v[132:135], v37 offset:43008
  v_mfma_f32_16x16x32_bf16 v[64:67], v[200:203], v[136:139], v[64:67]
  v_mfma_f32_16x16x32_bf16 v[80:83], v[136:139], v[204:207], v[80:83]
  ds_read_b128 v[136:139], v37 offset:45056
  v_mfma_f32_16x16x32_bf16 v[68:71], v[200:203], v[172:175], v[68:71]
  v_mfma_f32_16x16x32_bf16 v[84:87], v[172:175], v[204:207], v[84:87]
  ds_read_b128 v[172:175], v37 offset:47104
  s_waitcnt lgkmcnt(7)
  v_mfma_f32_16x16x32_bf16 v[96:99], v[148:151], v[148:151], v[96:99]
  v_mfma_f32_16x16x32_bf16 v[124:127], v[44:47], v[148:151], v[124:127]
  s_waitcnt lgkmcnt(6)
  v_mfma_f32_16x16x32_bf16 v[96:99], v[152:155], v[152:155], v[96:99]
  v_mfma_f32_16x16x32_bf16 v[124:127], v[44:47], v[152:155], v[124:127]
  v_mfma_f32_16x16x32_bf16 v[56:59], v[208:211], v[176:179], v[56:59]
  v_mfma_f32_16x16x32_bf16 v[72:75], v[176:179], v[212:215], v[72:75]
  ds_read_b128 v[176:179], v38 offset:40960
  v_mfma_f32_16x16x32_bf16 v[60:63], v[208:211], v[180:183], v[60:63]
  v_mfma_f32_16x16x32_bf16 v[76:79], v[180:183], v[212:215], v[76:79]
  ds_read_b128 v[180:183], v38 offset:43008
  s_barrier
  v_mfma_f32_16x16x32_bf16 v[64:67], v[208:211], v[184:187], v[64:67]
  ds_read_b64_tr_b16 v[200:201], v39 offset:0
  ds_read_b64_tr_b16 v[202:203], v39 offset:512
  v_mfma_f32_16x16x32_bf16 v[80:83], v[184:187], v[212:215], v[80:83]
  ds_read_b64_tr_b16 v[204:205], v39 offset:8192
  ds_read_b64_tr_b16 v[206:207], v39 offset:8704
  v_mfma_f32_16x16x32_bf16 v[68:71], v[208:211], v[188:191], v[68:71]
  ds_read_b64_tr_b16 v[216:217], v39 offset:4096
  ds_read_b64_tr_b16 v[218:219], v39 offset:4608
  v_mfma_f32_16x16x32_bf16 v[84:87], v[188:191], v[212:215], v[84:87]
  ds_read_b64_tr_b16 v[220:221], v39 offset:12288
  ds_read_b64_tr_b16 v[222:223], v39 offset:12800
  v_mfma_f32_16x16x32_bf16 v[88:91], v[208:211], v[44:47], v[88:91]
  v_mfma_f32_16x16x32_bf16 v[92:95], v[44:47], v[212:215], v[92:95]
  s_waitcnt lgkmcnt(6)
  v_mfma_f32_16x16x32_bf16 v[88:91], v[200:203], v[44:47], v[88:91]
  ds_read_b128 v[148:151], v164 offset:49152
  s_waitcnt lgkmcnt(5)
  v_mfma_f32_16x16x32_bf16 v[92:95], v[44:47], v[204:207], v[92:95]
  ds_read_b128 v[152:155], v165 offset:49152
  v_mfma_f32_16x16x32_bf16 v[56:59], v[200:203], v[128:131], v[56:59]
  ds_read_b128 v[184:187], v38 offset:53248
  v_mfma_f32_16x16x32_bf16 v[72:75], v[128:131], v[204:207], v[72:75]
  ds_read_b128 v[128:131], v37 offset:49152
  v_mfma_f32_16x16x32_bf16 v[60:63], v[200:203], v[132:135], v[60:63]
  ds_read_b128 v[188:191], v38 offset:55296
  v_mfma_f32_16x16x32_bf16 v[76:79], v[132:135], v[204:207], v[76:79]
  ds_read_b128 v[132:135], v37 offset:51200
  v_mfma_f32_16x16x32_bf16 v[64:67], v[200:203], v[136:139], v[64:67]
  v_mfma_f32_16x16x32_bf16 v[80:83], v[136:139], v[204:207], v[80:83]
  ds_read_b128 v[136:139], v37 offset:53248
  v_mfma_f32_16x16x32_bf16 v[68:71], v[200:203], v[172:175], v[68:71]
  v_mfma_f32_16x16x32_bf16 v[84:87], v[172:175], v[204:207], v[84:87]
  ds_read_b128 v[172:175], v37 offset:55296
  s_waitcnt lgkmcnt(7)
  v_mfma_f32_16x16x32_bf16 v[96:99], v[148:151], v[148:151], v[96:99]
  v_mfma_f32_16x16x32_bf16 v[124:127], v[44:47], v[148:151], v[124:127]
  s_waitcnt lgkmcnt(6)
  v_mfma_f32_16x16x32_bf16 v[96:99], v[152:155], v[152:155], v[96:99]
  v_mfma_f32_16x16x32_bf16 v[124:127], v[44:47], v[152:155], v[124:127]
  v_mfma_f32_16x16x32_bf16 v[56:59], v[216:219], v[176:179], v[56:59]
  v_mfma_f32_16x16x32_bf16 v[72:75], v[176:179], v[220:223], v[72:75]
  ds_read_b128 v[176:179], v38 offset:49152
  v_mfma_f32_16x16x32_bf16 v[60:63], v[216:219], v[180:183], v[60:63]
  v_mfma_f32_16x16x32_bf16 v[76:79], v[180:183], v[220:223], v[76:79]
  ds_read_b128 v[180:183], v38 offset:51200
  s_barrier
  v_mfma_f32_16x16x32_bf16 v[64:67], v[216:219], v[192:195], v[64:67]
  ds_read_b128 v[148:151], v164 offset:57344
  ds_read_b128 v[152:155], v165 offset:57344
  v_mfma_f32_16x16x32_bf16 v[80:83], v[192:195], v[220:223], v[80:83]
  ds_read_b64_tr_b16 v[200:201], v39 offset:16384
  ds_read_b64_tr_b16 v[202:203], v39 offset:16896
  v_mfma_f32_16x16x32_bf16 v[68:71], v[216:219], v[196:199], v[68:71]
  ds_read_b64_tr_b16 v[204:205], v39 offset:24576
  ds_read_b64_tr_b16 v[206:207], v39 offset:25088
  v_mfma_f32_16x16x32_bf16 v[84:87], v[196:199], v[220:223], v[84:87]
  ds_read_b64_tr_b16 v[208:209], v39 offset:20480
  ds_read_b64_tr_b16 v[210:211], v39 offset:20992
  v_mfma_f32_16x16x32_bf16 v[88:91], v[216:219], v[44:47], v[88:91]
  ds_read_b64_tr_b16 v[212:213], v39 offset:28672
  ds_read_b64_tr_b16 v[214:215], v39 offset:29184
  v_mfma_f32_16x16x32_bf16 v[92:95], v[44:47], v[220:223], v[92:95]
  s_waitcnt lgkmcnt(9)
  v_mfma_f32_16x16x32_bf16 v[96:99], v[148:151], v[148:151], v[96:99]
  v_mfma_f32_16x16x32_bf16 v[124:127], v[44:47], v[148:151], v[124:127]
  s_waitcnt lgkmcnt(8)
  v_mfma_f32_16x16x32_bf16 v[96:99], v[152:155], v[152:155], v[96:99]
  v_mfma_f32_16x16x32_bf16 v[124:127], v[44:47], v[152:155], v[124:127]
  s_waitcnt lgkmcnt(6)
  v_mfma_f32_16x16x32_bf16 v[88:91], v[200:203], v[44:47], v[88:91]
  s_waitcnt lgkmcnt(4)
  v_mfma_f32_16x16x32_bf16 v[92:95], v[44:47], v[204:207], v[92:95]
  v_mfma_f32_16x16x32_bf16 v[56:59], v[200:203], v[128:131], v[56:59]
  ds_read_b128 v[192:195], v38 offset:61440
  v_mfma_f32_16x16x32_bf16 v[72:75], v[128:131], v[204:207], v[72:75]
  ds_read_b128 v[128:131], v37 offset:57344
  v_mfma_f32_16x16x32_bf16 v[60:63], v[200:203], v[132:135], v[60:63]
  ds_read_b128 v[196:199], v38 offset:63488
  v_cndmask_b32_e64 v156, v96, v97, s[68:69]
  v_cndmask_b32_e64 v157, v98, v99, s[68:69]
  v_mfma_f32_16x16x32_bf16 v[76:79], v[132:135], v[204:207], v[76:79]
  ds_read_b128 v[132:135], v37 offset:59392
  v_mul_f32 v158, 0xbb000000, v124
  v_cndmask_b32_e64 v156, v156, v157, s[64:65]
  v_mfma_f32_16x16x32_bf16 v[64:67], v[200:203], v[136:139], v[64:67]
  v_mul_f32 v156, 0x3b000000, v156
  v_fma_f32 v156, -v158, v158, v156
  v_mfma_f32_16x16x32_bf16 v[80:83], v[136:139], v[204:207], v[80:83]
  ds_read_b128 v[136:139], v37 offset:61440
  v_max_f32 v156, 0, v156
  v_add_f32 v156, 0x3727c5ac, v156
  v_mfma_f32_16x16x32_bf16 v[68:71], v[200:203], v[172:175], v[68:71]
  v_rsq_f32 v156, v156
  s_nop 0
  ds_write2st64_b32 v166, v156, v158 offset0:0 offset1:1
  v_mfma_f32_16x16x32_bf16 v[84:87], v[172:175], v[204:207], v[84:87]
  ds_read_b128 v[172:175], v37 offset:63488
  s_waitcnt lgkmcnt(9)
  v_mfma_f32_16x16x32_bf16 v[56:59], v[208:211], v[176:179], v[56:59]
  s_waitcnt lgkmcnt(7)
  v_mfma_f32_16x16x32_bf16 v[72:75], v[176:179], v[212:215], v[72:75]
  ds_read_b128 v[176:179], v38 offset:57344
  v_mfma_f32_16x16x32_bf16 v[60:63], v[208:211], v[180:183], v[60:63]
  v_mfma_f32_16x16x32_bf16 v[76:79], v[180:183], v[212:215], v[76:79]
  ds_read_b128 v[180:183], v38 offset:59392
  s_waitcnt lgkmcnt(0)
  s_barrier
  v_mfma_f32_16x16x32_bf16 v[64:67], v[208:211], v[184:187], v[64:67]
  ds_read_b64_tr_b16 v[200:201], v39 offset:32768
  ds_read_b64_tr_b16 v[202:203], v39 offset:33280
  v_mfma_f32_16x16x32_bf16 v[80:83], v[184:187], v[212:215], v[80:83]
  ds_read_b64_tr_b16 v[204:205], v40 offset:0
  ds_read_b64_tr_b16 v[206:207], v40 offset:512
  v_mfma_f32_16x16x32_bf16 v[68:71], v[208:211], v[188:191], v[68:71]
  ds_read_b64_tr_b16 v[216:217], v39 offset:36864
  ds_read_b64_tr_b16 v[218:219], v39 offset:37376
  v_mfma_f32_16x16x32_bf16 v[84:87], v[188:191], v[212:215], v[84:87]
  ds_read_b64_tr_b16 v[220:221], v40 offset:4096
  ds_read_b64_tr_b16 v[222:223], v40 offset:4608
  v_mfma_f32_16x16x32_bf16 v[88:91], v[208:211], v[44:47], v[88:91]
  v_mfma_f32_16x16x32_bf16 v[92:95], v[44:47], v[212:215], v[92:95]
  s_waitcnt lgkmcnt(6)
  v_mfma_f32_16x16x32_bf16 v[88:91], v[200:203], v[44:47], v[88:91]
  ds_read2_b32 v[184:185], v167 offset0:0 offset1:64
  ds_read2_b32 v[186:187], v167 offset0:16 offset1:80
  ds_read2_b32 v[188:189], v167 offset0:32 offset1:96
  s_waitcnt lgkmcnt(7)
  v_mfma_f32_16x16x32_bf16 v[92:95], v[44:47], v[204:207], v[92:95]
  ds_read2_b32 v[190:191], v167 offset0:48 offset1:112
  ds_read_b128 v[224:227], v168 offset:256
  ds_read_b128 v[96:99], v168 offset:0
  s_waitcnt lgkmcnt(8)
  v_mfma_f32_16x16x32_bf16 v[88:91], v[216:219], v[44:47], v[88:91]
  ds_read_b128 v[228:231], v168 offset:320
  ds_read_b128 v[124:127], v168 offset:64
  ds_read_b128 v[232:235], v168 offset:384
  s_waitcnt lgkmcnt(9)
  v_mfma_f32_16x16x32_bf16 v[92:95], v[44:47], v[220:223], v[92:95]
  ds_read_b128 v[208:211], v168 offset:128
  ds_read_b128 v[236:239], v168 offset:448
  ds_read_b128 v[212:215], v168 offset:192
  v_mfma_f32_16x16x32_bf16 v[56:59], v[200:203], v[128:131], v[56:59]
  v_mfma_f32_16x16x32_bf16 v[60:63], v[200:203], v[132:135], v[60:63]
  v_mfma_f32_16x16x32_bf16 v[56:59], v[216:219], v[176:179], v[56:59]
  v_mfma_f32_16x16x32_bf16 v[60:63], v[216:219], v[180:183], v[60:63]
  v_mfma_f32_16x16x32_bf16 v[72:75], v[128:131], v[204:207], v[72:75]
  v_mov_b32_dpp v88, v88 quad_perm:[0,0,0,0] row_mask:0xf bank_mask:0xf
  v_mov_b32_dpp v89, v89 quad_perm:[0,0,0,0] row_mask:0xf bank_mask:0xf
  v_mfma_f32_16x16x32_bf16 v[76:79], v[132:135], v[204:207], v[76:79]
  v_mov_b32_dpp v90, v90 quad_perm:[0,0,0,0] row_mask:0xf bank_mask:0xf
  v_mov_b32_dpp v91, v91 quad_perm:[0,0,0,0] row_mask:0xf bank_mask:0xf
  v_mfma_f32_16x16x32_bf16 v[72:75], v[176:179], v[220:223], v[72:75]
  v_mfma_f32_16x16x32_bf16 v[76:79], v[180:183], v[220:223], v[76:79]
  s_waitcnt lgkmcnt(11)
  v_fmac_f32 v56, v185, v88
  v_fmac_f32 v57, v185, v89
  v_mfma_f32_16x16x32_bf16 v[64:67], v[200:203], v[136:139], v[64:67]
  v_fmac_f32 v58, v185, v90
  v_fmac_f32 v59, v185, v91
  v_mul_f32 v56, v184, v56
  v_mfma_f32_16x16x32_bf16 v[68:71], v[200:203], v[172:175], v[68:71]
  v_mul_f32 v57, v184, v57
  v_mul_f32 v58, v184, v58
  v_mul_f32 v59, v184, v59
  v_mfma_f32_16x16x32_bf16 v[64:67], v[216:219], v[192:195], v[64:67]
  s_waitcnt lgkmcnt(10)
  v_fmac_f32 v60, v187, v88
  v_fmac_f32 v61, v187, v89
  v_fmac_f32 v62, v187, v90
  v_mfma_f32_16x16x32_bf16 v[68:71], v[216:219], v[196:199], v[68:71]
  v_fmac_f32 v63, v187, v91
  v_mul_f32 v60, v186, v60
  v_mul_f32 v61, v186, v61
  v_mfma_f32_16x16x32_bf16 v[80:83], v[136:139], v[204:207], v[80:83]
  v_mul_f32 v62, v186, v62
  v_mul_f32 v63, v186, v63
  v_cvt_pk_bf16_f32 v32, v56, v57
  v_mfma_f32_16x16x32_bf16 v[84:87], v[172:175], v[204:207], v[84:87]
  v_cvt_pk_bf16_f32 v34, v60, v61
  v_cvt_pk_bf16_f32 v33, v58, v59
  v_cvt_pk_bf16_f32 v35, v62, v63
  v_mfma_f32_16x16x32_bf16 v[80:83], v[192:195], v[220:223], v[80:83]
  v_permlane16_swap_b32 v32, v34
  v_permlane16_swap_b32 v33, v35
  buffer_store_dwordx4 v[32:35], v2, s[36:39], s53 offen offset:0 sc1
  v_mfma_f32_16x16x32_bf16 v[84:87], v[196:199], v[220:223], v[84:87]
  s_barrier
  s_waitcnt lgkmcnt(7)
  v_fmac_f32 v72, v224, v92
  v_fmac_f32 v73, v225, v92
  v_fmac_f32 v74, v226, v92
  v_fmac_f32 v75, v227, v92
  s_waitcnt lgkmcnt(6)
  v_mul_f32 v72, v96, v72
  v_mul_f32 v73, v97, v73
  v_mul_f32 v74, v98, v74
  v_mul_f32 v75, v99, v75
  s_waitcnt lgkmcnt(5)
  v_fmac_f32 v76, v228, v92
  v_fmac_f32 v77, v229, v92
  v_fmac_f32 v78, v230, v92
  v_fmac_f32 v79, v231, v92
  s_waitcnt lgkmcnt(4)
  v_mul_f32 v76, v124, v76
  v_mul_f32 v77, v125, v77
  v_mul_f32 v78, v126, v78
  v_mul_f32 v79, v127, v79
  v_cvt_pk_bf16_f32 v152, v72, v73
  v_cvt_pk_bf16_f32 v154, v76, v77
  v_cvt_pk_bf16_f32 v153, v74, v75
  v_cvt_pk_bf16_f32 v155, v78, v79
  v_permlane32_swap_b32 v152, v154
  s_nop 0
  v_permlane32_swap_b32 v153, v155
  buffer_store_dwordx4 v[152:155], v2, s[36:39], s53 offen offset:1024 sc1
  ds_write_b128 v169, v[152:155] offset:1024
  s_mov_b64 exec, s[66:67]
  ds_read_b128 v[48:51], v170 offset:1024
  ds_read_b128 v[52:55], v170 offset:1536
  s_mov_b64 exec, -1
  v_fmac_f32 v64, v189, v88
  v_fmac_f32 v65, v189, v89
  v_fmac_f32 v66, v189, v90
  v_fmac_f32 v67, v189, v91
  v_mul_f32 v64, v188, v64
  v_mul_f32 v65, v188, v65
  v_mul_f32 v66, v188, v66
  v_mul_f32 v67, v188, v67
  v_fmac_f32 v68, v191, v88
  v_fmac_f32 v69, v191, v89
  v_fmac_f32 v70, v191, v90
  v_fmac_f32 v71, v191, v91
  v_mul_f32 v68, v190, v68
  v_mul_f32 v69, v190, v69
  v_mul_f32 v70, v190, v70
  v_mul_f32 v71, v190, v71
  v_cvt_pk_bf16_f32 v148, v64, v65
  v_cvt_pk_bf16_f32 v150, v68, v69
  v_cvt_pk_bf16_f32 v149, v66, v67
  v_cvt_pk_bf16_f32 v151, v70, v71
  v_permlane16_swap_b32 v148, v150
  s_nop 0
  v_permlane16_swap_b32 v149, v151
  buffer_store_dwordx4 v[148:151], v2, s[36:39], s53 offen offset:2304 sc1
  ds_write_b128 v169, v[148:151]
  v_bitop3_b32 v160, v32, v33, v34 bitop3:0x96
  s_waitcnt lgkmcnt(2)
  v_bitop3_b32 v162, v48, v49, v50 bitop3:0x96
  s_waitcnt lgkmcnt(1)
  v_bitop3_b32 v163, v52, v53, v54 bitop3:0x96
  v_add3_u32 v160, v160, v162, v163
  v_bitop3_b32 v162, v35, v51, v55 bitop3:0x96
  v_add3_u32 v160, v160, v162, s50
  buffer_store_dword v160, v3, s[36:39], s53 offen offset:2048 sc1
  s_barrier
  v_fmac_f32 v80, v232, v92
  v_fmac_f32 v81, v233, v92
  v_fmac_f32 v82, v234, v92
  v_fmac_f32 v83, v235, v92
  v_mul_f32 v80, v208, v80
  v_mul_f32 v81, v209, v81
  v_mul_f32 v82, v210, v82
  v_mul_f32 v83, v211, v83
  v_fmac_f32 v84, v236, v92
  v_fmac_f32 v85, v237, v92
  v_fmac_f32 v86, v238, v92
  v_fmac_f32 v87, v239, v92
  v_mul_f32 v84, v212, v84
  v_mul_f32 v85, v213, v85
  v_mul_f32 v86, v214, v86
  v_mul_f32 v87, v215, v87
  v_cvt_pk_bf16_f32 v156, v80, v81
  v_cvt_pk_bf16_f32 v158, v84, v85
  v_cvt_pk_bf16_f32 v157, v82, v83
  v_cvt_pk_bf16_f32 v159, v86, v87
  v_permlane32_swap_b32 v156, v158
  s_nop 0
  v_permlane32_swap_b32 v157, v159
  buffer_store_dwordx4 v[156:159], v2, s[36:39], s53 offen offset:3328 sc1
  ds_write_b128 v169, v[156:159] offset:2048
  s_mov_b64 exec, s[66:67]
  ds_read_b128 v[140:143], v170 offset:2048
  ds_read_b128 v[144:147], v170 offset:2560
  s_mov_b64 exec, -1
  s_add_u32 s56, s53, 2304
  s_cmp_lg_u32 s70, 0
  s_cselect_b32 s55, -1, 0
  v_and_b32 v64, s55, v22
  v_and_b32 v65, s55, v22
  v_mov_b64 v[66:67], v[64:65]
  v_mov_b64 v[68:69], v[64:65]
  v_mov_b64 v[70:71], v[64:65]
  s_cmp_lg_u32 s71, 0
  s_cselect_b32 s55, -1, 0
  v_and_b32 v72, s55, v22
  v_and_b32 v73, s55, v22
  v_mov_b64 v[74:75], v[72:73]
  v_mov_b64 v[76:77], v[72:73]
  v_mov_b64 v[78:79], v[72:73]
  v_bitop3_b32 v161, v148, v149, v150 bitop3:0x96
  s_waitcnt lgkmcnt(1)
  v_bitop3_b32 v162, v140, v141, v142 bitop3:0x96
  s_waitcnt lgkmcnt(0)
  v_bitop3_b32 v163, v144, v145, v146 bitop3:0x96
  v_add3_u32 v161, v161, v162, v163
  v_bitop3_b32 v162, v151, v143, v147 bitop3:0x96
  v_add3_u32 v161, v161, v162, s50
  buffer_store_dword v161, v3, s[36:39], s56 offen offset:2048 sc1
  s_cmp_lg_u32 s70, 0
  s_cselect_b32 s42, s38, 0
  buffer_load_dwordx4 v[88:91], v2, s[40:43], s70 offen sc1
  s_mov_b64 exec, s[66:67]
  buffer_load_dwordx4 v[64:67], v4, s[40:43], s70 offen offset:1024 sc1
  buffer_load_dwordx4 v[68:71], v4, s[40:43], s70 offen offset:1536 sc1
  s_mov_b64 exec, -1
  buffer_load_dword v28, v3, s[40:43], s70 offen offset:2048 sc1
  s_cmp_lg_u32 s71, 0
  s_cselect_b32 s42, s38, 0
  buffer_load_dwordx4 v[92:95], v2, s[40:43], s71 offen sc1
  s_mov_b64 exec, s[66:67]
  buffer_load_dwordx4 v[72:75], v4, s[40:43], s71 offen offset:1024 sc1
  buffer_load_dwordx4 v[76:79], v4, s[40:43], s71 offen offset:1536 sc1
  s_mov_b64 exec, -1
  buffer_load_dword v29, v3, s[40:43], s71 offen offset:2048 sc1
  s_cmp_lg_u32 s72, 0
  s_cselect_b32 s55, -1, 0
  v_and_b32 v80, s55, v22
  v_and_b32 v81, s55, v22
  v_mov_b64 v[82:83], v[80:81]
  v_mov_b64 v[84:85], v[80:81]
  v_mov_b64 v[86:87], v[80:81]
  s_cmp_lg_u32 s73, 0
  s_cselect_b32 s55, -1, 0
  v_and_b32 v56, s55, v22
  v_and_b32 v57, s55, v22
  v_mov_b64 v[58:59], v[56:57]
  v_mov_b64 v[60:61], v[56:57]
  v_mov_b64 v[62:63], v[56:57]
  s_cmp_lg_u32 s72, 0
  s_cselect_b32 s42, s38, 0
  buffer_load_dwordx4 v[96:99], v2, s[40:43], s72 offen sc1
  s_mov_b64 exec, s[66:67]
  buffer_load_dwordx4 v[80:83], v4, s[40:43], s72 offen offset:1024 sc1
  buffer_load_dwordx4 v[84:87], v4, s[40:43], s72 offen offset:1536 sc1
  s_mov_b64 exec, -1
  buffer_load_dword v30, v3, s[40:43], s72 offen offset:2048 sc1
  s_cmp_lg_u32 s73, 0
  s_cselect_b32 s42, s38, 0
  buffer_load_dwordx4 v[36:39], v2, s[40:43], s73 offen sc1
  s_mov_b64 exec, s[66:67]
  buffer_load_dwordx4 v[56:59], v4, s[40:43], s73 offen offset:1024 sc1
  buffer_load_dwordx4 v[60:63], v4, s[40:43], s73 offen offset:1536 sc1
  s_mov_b64 exec, -1
  buffer_load_dword v31, v3, s[40:43], s73 offen offset:2048 sc1
  v_mov_b64 v[124:125], 0
  v_mov_b64 v[126:127], 0
  v_mov_b64 v[128:129], 0
  v_mov_b64 v[130:131], 0
  v_mov_b64 v[132:133], 0
  v_mov_b64 v[134:135], 0
  v_mov_b64 v[136:137], 0
  v_mov_b64 v[138:139], 0
  ds_read_u16_d16_hi v124, v17 offset:0
  ds_read_u16_d16_hi v125, v17 offset:128
  v_xor_b32 v17, 32, v17
  ds_read_u16_d16_hi v128, v17 offset:0
  ds_read_u16_d16_hi v129, v17 offset:128
  v_xor_b32 v17, 64, v17
  ds_read_u16_d16_hi v136, v17 offset:0
  ds_read_u16_d16_hi v137, v17 offset:128
  v_xor_b32 v17, 32, v17
  ds_read_u16_d16_hi v132, v17 offset:0
  ds_read_u16_d16_hi v133, v17 offset:128
  s_add_u32 s55, s52, 98304
  v_add_u32 v232, s55, v2
  s_waitcnt lgkmcnt(0)
  s_barrier
  ds_read_b128 v[44:47], v232 offset:1024
  ds_read_b128 v[40:43], v232 offset:0
; query tile 1 against this block's key tile 0 needs no neighbour
  s_waitcnt lgkmcnt(1)
  v_mfma_f32_32x32x16_bf16 v[140:155], v[32:35], v[44:47], 0
  s_nop 11
  v_exp_f32 v140, v140
  v_exp_f32 v141, v141
  v_exp_f32 v142, v142
  v_exp_f32 v143, v143
  v_exp_f32 v144, v144
  v_exp_f32 v145, v145
  v_exp_f32 v146, v146
  v_exp_f32 v147, v147
  v_cvt_pk_bf16_f32 v204, v140, v141
  v_cvt_pk_bf16_f32 v205, v142, v143
  v_cvt_pk_bf16_f32 v206, v144, v145
  v_cvt_pk_bf16_f32 v207, v146, v147
  v_exp_f32 v148, v148
  v_exp_f32 v149, v149
  v_mfma_f32_32x32x16_bf16 v[172:187], v[48:51], v[204:207], 0
  v_exp_f32 v150, v150
  v_exp_f32 v151, v151
  v_exp_f32 v152, v152
  v_exp_f32 v153, v153
  v_exp_f32 v154, v154
  v_exp_f32 v155, v155
  v_cvt_pk_bf16_f32 v208, v148, v149
  v_cvt_pk_bf16_f32 v209, v150, v151
  v_cvt_pk_bf16_f32 v210, v152, v153
  v_cvt_pk_bf16_f32 v211, v154, v155
  s_nop 1
  v_mfma_f32_32x32x16_bf16 v[172:187], v[52:55], v[208:211], v[172:187]
  s_waitcnt lgkmcnt(0)
.Lfb_att:
; ==== attention: wave (hd, p) owns the key tiles of parity p for both query tiles
  s_cmp_lg_u32 s70, 0
  s_cselect_b32 s55, s50, 0
  s_waitcnt vmcnt(15)
  v_bitop3_b32 v229, v88, v89, v90 bitop3:0x96
  s_waitcnt vmcnt(14)
  v_bitop3_b32 v230, v64, v65, v66 bitop3:0x96
  s_waitcnt vmcnt(13)
  v_bitop3_b32 v231, v68, v69, v70 bitop3:0x96
  v_add3_u32 v229, v229, v230, v231
  v_bitop3_b32 v230, v91, v67, v71 bitop3:0x96
  v_add3_u32 v229, v229, v230, s55
  s_waitcnt vmcnt(12)
  v_cmp_ne_u32 vcc, v229, v28
  s_cbranch_vccnz .Lfb_n1_retry
.Lfb_n1_ok:
  v_mfma_f32_32x32x16_bf16 v[140:155], v[88:91], v[44:47], 0
  v_mfma_f32_32x32x16_bf16 v[156:171], v[88:91], v[40:43], 0
  s_nop 10
  v_exp_f32 v140, v140
  v_exp_f32 v141, v141
  v_exp_f32 v142, v142
  v_exp_f32 v143, v143
  v_exp_f32 v144, v144
  v_exp_f32 v145, v145
  v_exp_f32 v146, v146
  v_exp_f32 v147, v147
  v_cvt_pk_bf16_f32 v204, v140, v141
  v_cvt_pk_bf16_f32 v205, v142, v143
  v_cvt_pk_bf16_f32 v206, v144, v145
  v_cvt_pk_bf16_f32 v207, v146, v147
  v_exp_f32 v148, v148
  v_exp_f32 v149, v149
  v_mfma_f32_32x32x16_bf16 v[172:187], v[64:67], v[204:207], v[172:187]
  s_xor_b32 m0, s74, 0
  s_xor_b32 s56, s75, 0
  buffer_load_dwordx4 v5, s[12:15], s56 offen lds
  v_exp_f32 v150, v150
  v_exp_f32 v151, v151
  v_exp_f32 v152, v152
  v_exp_f32 v153, v153
  s_cmp_lg_u32 s71, 0
  s_cselect_b32 s55, s50, 0
  s_waitcnt vmcnt(12)
  v_bitop3_b32 v229, v92, v93, v94 bitop3:0x96
  s_waitcnt vmcnt(11)
  v_bitop3_b32 v230, v72, v73, v74 bitop3:0x96
  s_waitcnt vmcnt(10)
  v_bitop3_b32 v231, v76, v77, v78 bitop3:0x96
  v_add3_u32 v229, v229, v230, v231
  v_bitop3_b32 v230, v95, v75, v79 bitop3:0x96
  v_add3_u32 v229, v229, v230, s55
  s_waitcnt vmcnt(9)
  v_cmp_ne_u32 vcc, v229, v29
  v_exp_f32 v154, v154
  v_exp_f32 v155, v155
  v_cvt_pk_bf16_f32 v208, v148, v149
  v_cvt_pk_bf16_f32 v209, v150, v151
  v_cvt_pk_bf16_f32 v210, v152, v153
  v_cvt_pk_bf16_f32 v211, v154, v155
  v_exp_f32 v156, v156
  v_exp_f32 v157, v157
  v_mfma_f32_32x32x16_bf16 v[172:187], v[68:71], v[208:211], v[172:187]
  s_cbranch_vccnz .Lfb_n2_retry
.Lfb_n2_ok:
  v_exp_f32 v158, v158
  v_exp_f32 v159, v159
  v_exp_f32 v160, v160
  v_exp_f32 v161, v161
  v_mfma_f32_32x32x16_bf16 v[140:155], v[92:95], v[44:47], 0
  v_exp_f32 v162, v162
  v_exp_f32 v163, v163
  v_cvt_pk_bf16_f32 v204, v156, v157
  v_cvt_pk_bf16_f32 v205, v158, v159
  v_cvt_pk_bf16_f32 v206, v160, v161
  v_cvt_pk_bf16_f32 v207, v162, v163
  v_exp_f32 v164, v164
  v_exp_f32 v165, v165
  v_mfma_f32_32x32x16_bf16 v[188:203], v[64:67], v[204:207], 0
  s_xor_b32 m0, s74, 8192
  s_xor_b32 s56, s75, 65536
  buffer_load_dwordx4 v5, s[12:15], s56 offen lds
  v_exp_f32 v166, v166
  v_exp_f32 v167, v167
  v_exp_f32 v168, v168
  v_exp_f32 v169, v169
  v_mfma_f32_32x32x16_bf16 v[212:227], v[32:35], v[40:43], 0
  v_exp_f32 v170, v170
  v_exp_f32 v171, v171
  v_cvt_pk_bf16_f32 v208, v164, v165
  v_cvt_pk_bf16_f32 v209, v166, v167
  v_cvt_pk_bf16_f32 v210, v168, v169
  v_cvt_pk_bf16_f32 v211, v170, v171
  v_exp_f32 v140, v140
  v_exp_f32 v141, v141
  v_mfma_f32_32x32x16_bf16 v[188:203], v[68:71], v[208:211], v[188:203]
  s_xor_b32 m0, s74, 16384
  s_xor_b32 s56, s75, 131072
  buffer_load_dwordx4 v5, s[12:15], s56 offen lds
  v_exp_f32 v142, v142
  v_exp_f32 v143, v143
  v_exp_f32 v144, v144
  v_exp_f32 v145, v145
  v_mfma_f32_32x32x16_bf16 v[156:171], v[92:95], v[40:43], 0
  v_exp_f32 v146, v146
  v_exp_f32 v147, v147
  v_cvt_pk_bf16_f32 v204, v140, v141
  v_cvt_pk_bf16_f32 v205, v142, v143
  v_cvt_pk_bf16_f32 v206, v144, v145
  v_cvt_pk_bf16_f32 v207, v146, v147
  v_exp_f32 v148, v148
  v_exp_f32 v149, v149
  v_mfma_f32_32x32x16_bf16 v[172:187], v[72:75], v[204:207], v[172:187]
  s_xor_b32 m0, s74, 24576
  s_xor_b32 s56, s75, 196608
  buffer_load_dwordx4 v5, s[12:15], s56 offen lds
  v_exp_f32 v150, v150
  v_exp_f32 v151, v151
  v_exp_f32 v152, v152
  v_exp_f32 v153, v153
  s_cmp_lg_u32 s72, 0
  s_cselect_b32 s55, s50, 0
  s_waitcnt vmcnt(11)
  v_bitop3_b32 v229, v96, v97, v98 bitop3:0x96
  s_waitcnt vmcnt(10)
  v_bitop3_b32 v230, v80, v81, v82 bitop3:0x96
  s_waitcnt vmcnt(9)
  v_bitop3_b32 v231, v84, v85, v86 bitop3:0x96
  v_add3_u32 v229, v229, v230, v231
  v_bitop3_b32 v230, v99, v83, v87 bitop3:0x96
  v_add3_u32 v229, v229, v230, s55
  s_waitcnt vmcnt(8)
  v_cmp_ne_u32 vcc, v229, v30
  v_exp_f32 v154, v154
  v_exp_f32 v155, v155
  v_cvt_pk_bf16_f32 v208, v148, v149
  v_cvt_pk_bf16_f32 v209, v150, v151
  v_cvt_pk_bf16_f32 v210, v152, v153
  v_cvt_pk_bf16_f32 v211, v154, v155
  v_exp_f32 v156, v156
  v_exp_f32 v157, v157
  v_mfma_f32_32x32x16_bf16 v[172:187], v[76:79], v[208:211], v[172:187]
  s_cbranch_vccnz .Lfb_n3_retry
.Lfb_n3_ok:
  s_xor_b32 m0, s74, 32768
  s_xor_b32 s56, s75, 262144
  buffer_load_dwordx4 v5, s[12:15], s56 offen lds
  v_exp_f32 v158, v158
  v_exp_f32 v159, v159
  v_exp_f32 v160, v160
  v_exp_f32 v161, v161
  v_mfma_f32_32x32x16_bf16 v[140:155], v[96:99], v[44:47], 0
  v_exp_f32 v162, v162
  v_exp_f32 v163, v163
  v_cvt_pk_bf16_f32 v204, v156, v157
  v_cvt_pk_bf16_f32 v205, v158, v159
  v_cvt_pk_bf16_f32 v206, v160, v161
  v_cvt_pk_bf16_f32 v207, v162, v163
  v_exp_f32 v164, v164
  v_exp_f32 v165, v165
  v_mfma_f32_32x32x16_bf16 v[188:203], v[72:75], v[204:207], v[188:203]
  s_xor_b32 m0, s74, 40960
  s_xor_b32 s56, s75, 327680
  buffer_load_dwordx4 v5, s[12:15], s56 offen lds
  v_exp_f32 v166, v166
  v_exp_f32 v167, v167
  v_exp_f32 v168, v168
  v_exp_f32 v169, v169
  v_exp_f32 v170, v170
  v_exp_f32 v171, v171
  v_cvt_pk_bf16_f32 v208, v164, v165
  v_cvt_pk_bf16_f32 v209, v166, v167
  v_cvt_pk_bf16_f32 v210, v168, v169
  v_cvt_pk_bf16_f32 v211, v170, v171
  v_exp_f32 v140, v140
  v_exp_f32 v141, v141
  v_mfma_f32_32x32x16_bf16 v[188:203], v[76:79], v[208:211], v[188:203]
  s_xor_b32 m0, s74, 49152
  s_xor_b32 s56, s75, 393216
  buffer_load_dwordx4 v5, s[12:15], s56 offen lds
  v_exp_f32 v142, v142
  v_exp_f32 v143, v143
  v_exp_f32 v144, v144
  v_exp_f32 v145, v145
  v_mfma_f32_32x32x16_bf16 v[156:171], v[96:99], v[40:43], 0
  v_exp_f32 v146, v146
  v_exp_f32 v147, v147
  v_cvt_pk_bf16_f32 v204, v140, v141
  v_cvt_pk_bf16_f32 v205, v142, v143
  v_cvt_pk_bf16_f32 v206, v144, v145
  v_cvt_pk_bf16_f32 v207, v146, v147
  v_exp_f32 v148, v148
  v_exp_f32 v149, v149
  v_mfma_f32_32x32x16_bf16 v[172:187], v[80:83], v[204:207], v[172:187]
  s_xor_b32 m0, s74, 57344
  s_xor_b32 s56, s75, 458752
  buffer_load_dwordx4 v5, s[12:15], s56 offen lds
  v_exp_f32 v150, v150
  v_exp_f32 v151, v151
  v_exp_f32 v152, v152
  v_exp_f32 v153, v153
  s_cmp_lg_u32 s73, 0
  s_cselect_b32 s55, s50, 0
  s_waitcnt vmcnt(11)
  v_bitop3_b32 v229, v36, v37, v38 bitop3:0x96
  s_waitcnt vmcnt(10)
  v_bitop3_b32 v230, v56, v57, v58 bitop3:0x96
  s_waitcnt vmcnt(9)
  v_bitop3_b32 v231, v60, v61, v62 bitop3:0x96
  v_add3_u32 v229, v229, v230, v231
  v_bitop3_b32 v230, v39, v59, v63 bitop3:0x96
  v_add3_u32 v229, v229, v230, s55
  s_waitcnt vmcnt(8)
  v_cmp_ne_u32 vcc, v229, v31
  v_exp_f32 v154, v154
  v_exp_f32 v155, v155
  v_cvt_pk_bf16_f32 v208, v148, v149
  v_cvt_pk_bf16_f32 v209, v150, v151
  v_cvt_pk_bf16_f32 v210, v152, v153
  v_cvt_pk_bf16_f32 v211, v154, v155
  v_exp_f32 v156, v156
  v_exp_f32 v157, v157
  v_mfma_f32_32x32x16_bf16 v[172:187], v[84:87], v[208:211], v[172:187]
  s_cbranch_vccnz .Lfb_n4_retry
.Lfb_n4_ok:
  s_add_u32 m0, s63, 122880
  s_nop 0
  buffer_load_dwordx4 v14, s[20:23], 0 offen lds
  v_exp_f32 v158, v158
  v_exp_f32 v159, v159
  v_exp_f32 v160, v160
  v_exp_f32 v161, v161
  v_mfma_f32_32x32x16_bf16 v[140:155], v[36:39], v[44:47], 0
  v_exp_f32 v162, v162
  v_exp_f32 v163, v163
  v_cvt_pk_bf16_f32 v204, v156, v157
  v_cvt_pk_bf16_f32 v205, v158, v159
  v_cvt_pk_bf16_f32 v206, v160, v161
  v_cvt_pk_bf16_f32 v207, v162, v163
  v_exp_f32 v164, v164
  v_exp_f32 v165, v165
  v_mfma_f32_32x32x16_bf16 v[188:203], v[80:83], v[204:207], v[188:203]
  s_add_u32 m0, s63, 131072
  s_nop 0
  buffer_load_dwordx4 v14, s[24:27], 0 offen lds
  v_exp_f32 v166, v166
  v_exp_f32 v167, v167
  v_exp_f32 v168, v168
  v_exp_f32 v169, v169
  v_mfma_f32_32x32x16_bf16 v[64:79], v[36:39], v[40:43], 0
  v_exp_f32 v170, v170
  v_exp_f32 v171, v171
  v_cvt_pk_bf16_f32 v208, v164, v165
  v_cvt_pk_bf16_f32 v209, v166, v167
  v_cvt_pk_bf16_f32 v210, v168, v169
  v_cvt_pk_bf16_f32 v211, v170, v171
  s_add_u32 m0, s63, 139264
  s_nop 0
  buffer_load_dwordx4 v15, s[28:31], 0 offen lds
  s_cmp_eq_u32 s46, 0
  v_mfma_f32_32x32x16_bf16 v[188:203], v[84:87], v[208:211], v[188:203]
  s_cbranch_scc1 .Lfb_not6
; query tile 0 against the far tile of parity 1 (waves 4-7 only)
  v_exp_f32 v140, v140
  v_exp_f32 v141, v141
  v_exp_f32 v142, v142
  v_exp_f32 v143, v143
  v_exp_f32 v144, v144
  v_exp_f32 v145, v145
  v_exp_f32 v146, v146
  v_exp_f32 v147, v147
  v_cvt_pk_bf16_f32 v204, v140, v141
  v_cvt_pk_bf16_f32 v205, v142, v143
  v_cvt_pk_bf16_f32 v206, v144, v145
  v_cvt_pk_bf16_f32 v207, v146, v147
  v_exp_f32 v148, v148
  v_exp_f32 v149, v149
  v_mfma_f32_32x32x16_bf16 v[172:187], v[56:59], v[204:207], v[172:187]
  v_exp_f32 v150, v150
  v_exp_f32 v151, v151
  v_exp_f32 v152, v152
  v_exp_f32 v153, v153
  v_exp_f32 v154, v154
  v_exp_f32 v155, v155
  v_cvt_pk_bf16_f32 v208, v148, v149
  v_cvt_pk_bf16_f32 v209, v150, v151
  v_cvt_pk_bf16_f32 v210, v152, v153
  v_cvt_pk_bf16_f32 v211, v154, v155
  s_nop 1
  v_mfma_f32_32x32x16_bf16 v[172:187], v[60:63], v[208:211], v[172:187]
.Lfb_not6:
; the diagonal tile (key <= query) and the far tile (key > query - 256) are complementary triangles: one exp pass
  v_bitop3_b32 v156, v100, v212, v64 bitop3:0xca
  v_bitop3_b32 v157, v101, v213, v65 bitop3:0xca
  v_bitop3_b32 v158, v102, v214, v66 bitop3:0xca
  v_bitop3_b32 v159, v103, v215, v67 bitop3:0xca
  v_bitop3_b32 v160, v104, v216, v68 bitop3:0xca
  v_bitop3_b32 v161, v105, v217, v69 bitop3:0xca
  v_bitop3_b32 v162, v106, v218, v70 bitop3:0xca
  v_bitop3_b32 v163, v107, v219, v71 bitop3:0xca
  v_bitop3_b32 v164, v108, v220, v72 bitop3:0xca
  v_bitop3_b32 v165, v109, v221, v73 bitop3:0xca
  v_bitop3_b32 v166, v110, v222, v74 bitop3:0xca
  v_bitop3_b32 v167, v111, v223, v75 bitop3:0xca
  v_bitop3_b32 v168, v112, v224, v76 bitop3:0xca
  v_bitop3_b32 v169, v113, v225, v77 bitop3:0xca
  v_bitop3_b32 v170, v114, v226, v78 bitop3:0xca
  v_bitop3_b32 v171, v115, v227, v79 bitop3:0xca
  ds_write_b128 v6, v[172:175]
  ds_write_b128 v6, v[176:179] offset:1024
  ds_write_b32 v8, v180 offset:2048
  v_exp_f32 v156, v156
  v_exp_f32 v157, v157
  v_exp_f32 v158, v158
  v_exp_f32 v159, v159
  v_exp_f32 v160, v160
  v_exp_f32 v161, v161
  v_exp_f32 v162, v162
  v_exp_f32 v163, v163
  s_waitcnt lgkmcnt(0)
  s_barrier
  ds_read_b128 v[220:223], v7 offset:0
  ds_read_b128 v[224:227], v7 offset:1024
  ds_read_b32 v228, v9 offset:2048
  v_cvt_pk_bf16_f32 v204, v156, v157
  v_cvt_pk_bf16_f32 v205, v158, v159
  v_cvt_pk_bf16_f32 v206, v160, v161
  v_cvt_pk_bf16_f32 v207, v162, v163
  v_bitop3_b32 v212, v204, v116, 0 bitop3:0x30
  v_bitop3_b32 v213, v205, v117, 0 bitop3:0x30
  v_bitop3_b32 v214, v206, v118, 0 bitop3:0x30
  v_bitop3_b32 v215, v207, v119, 0 bitop3:0x30
  v_and_b32 v204, v116, v204
  v_and_b32 v205, v117, v205
  v_and_b32 v206, v118, v206
  v_and_b32 v207, v119, v207
  v_exp_f32 v164, v164
  v_exp_f32 v165, v165
  v_exp_f32 v166, v166
  v_exp_f32 v167, v167
  s_waitcnt lgkmcnt(2)
  v_add_f32 v188, v188, v220
  v_add_f32 v189, v189, v221
  v_add_f32 v190, v190, v222
  v_add_f32 v191, v191, v223
  s_waitcnt lgkmcnt(1)
  v_add_f32 v192, v192, v224
  v_add_f32 v193, v193, v225
  v_add_f32 v194, v194, v226
  v_add_f32 v195, v195, v227
  s_waitcnt lgkmcnt(0)
  v_add_f32 v196, v196, v228
  v_exp_f32 v168, v168
  v_exp_f32 v169, v169
  v_mfma_f32_32x32x16_bf16 v[188:203], v[56:59], v[212:215], v[188:203]
  v_exp_f32 v170, v170
  v_exp_f32 v171, v171
  v_mfma_f32_32x32x16_bf16 v[188:203], v[48:51], v[204:207], v[188:203]
  v_cvt_pk_bf16_f32 v208, v164, v165
  v_cvt_pk_bf16_f32 v209, v166, v167
  v_cvt_pk_bf16_f32 v210, v168, v169
  v_cvt_pk_bf16_f32 v211, v170, v171
  v_bitop3_b32 v216, v208, v120, 0 bitop3:0x30
  v_bitop3_b32 v217, v209, v121, 0 bitop3:0x30
  v_bitop3_b32 v218, v210, v122, 0 bitop3:0x30
  v_bitop3_b32 v219, v211, v123, 0 bitop3:0x30
  v_and_b32 v208, v120, v208
  v_and_b32 v209, v121, v209
  v_mfma_f32_32x32x16_bf16 v[188:203], v[60:63], v[216:219], v[188:203]
  v_and_b32 v210, v122, v210
  v_and_b32 v211, v123, v211
  s_mov_b32 s55, 528482304
  s_mov_b32 s56, 1073741824
  v_mfma_f32_32x32x16_bf16 v[188:203], v[52:55], v[208:211], v[188:203]
  s_nop 11
  v_rcp_f32 v230, v196
  v_subrev_u32 v229, s55, v196
  v_pk_mul_f32 v[188:189], v[188:189], v[230:231] op_sel_hi:[1,0]
  v_pk_mul_f32 v[190:191], v[190:191], v[230:231] op_sel_hi:[1,0]
  v_pk_mul_f32 v[192:193], v[192:193], v[230:231] op_sel_hi:[1,0]
  v_pk_mul_f32 v[194:195], v[194:195], v[230:231] op_sel_hi:[1,0]
  v_cvt_pk_bf16_f32 v220, v188, v189
  v_cvt_pk_bf16_f32 v221, v190, v191
  v_cvt_pk_bf16_f32 v222, v192, v193
  v_cvt_pk_bf16_f32 v223, v194, v195
  buffer_store_dwordx4 v[220:223], v10, s[36:39], s62 offen sc1
; the pass above used 0 as the softmax reference: exact while 2^-64 <= l <= 2^64, otherwise redo against the true row maximum
  v_cmp_lt_u32 vcc, s56, v229
  s_cbranch_vccnz .Lfb_slow
.Lfb_slow_done:
  v_bitop3_b32 v229, v220, v221, v222 bitop3:0x96
  v_add_u32 v229, v229, v223
  s_lshl2_add_u32 s55, s47, s45
  s_lshl1_add_u32 s55, s55, s46
  v_add_u32_dpp v229, v229, v229 quad_perm:[1,0,3,2] row_mask:0xf bank_mask:0xf
  ds_write_b32 v11, v229
  s_lshl_b32 s55, s55, 13
  s_add_u32 s55, s55, 5242880
  s_lshl1_add_u32 s56, s47, s46
  s_lshl4_add_u32 s56, s56, s45
  s_lshl_b32 s56, s56, 7
  s_add_u32 s56, s56, 7340032
  s_setprio 0
  s_waitcnt vmcnt(4) lgkmcnt(0)
  s_barrier
  s_cmp_eq_u32 s44, 7
  s_cbranch_scc0 .Lfb_noc
  v_lshlrev_b32 v4, 3, v1
  v_add_u32 v6, 161280, v4
  ds_read2st64_b64 v[6:9], v6 offset0:0 offset1:1
  s_lshl3_add_u32 s57, s47, s48
  s_lshl_b32 s57, s57, 9
  s_add_u32 s57, s57, 7340032
  s_waitcnt lgkmcnt(0)
  v_add3_u32 v6, v6, v8, s50
  v_add3_u32 v7, v7, v9, s50
  buffer_store_dwordx2 v[6:7], v4, s[36:39], s57 offen sc1
.Lfb_noc:
; ==== out projection: tile 64 rows x 64 columns; wave (rho, kap) = 16 rows x K half
  ds_read_b64_tr_b16 v[14:15], v24 offset:0
  s_and_b32 s57, s48, 3
  ds_read_b64_tr_b16 v[16:17], v24 offset:1024
  s_lshl_b32 s57, s57, 9
  ds_read_b64_tr_b16 v[28:29], v25 offset:0
  s_lshl2_add_u32 s70, s57, s55
  ds_read_b64_tr_b16 v[30:31], v25 offset:1024
  s_add_u32 s57, s57, s56
  ds_read_b64_tr_b16 v[32:33], v26 offset:0
  s_xor_b32 s71, s70, 2048
  ds_read_b64_tr_b16 v[34:35], v26 offset:1024
  s_xor_b32 s58, s57, 512
  ds_read_b64_tr_b16 v[36:37], v27 offset:0
  s_xor_b32 s72, s70, 4096
  ds_read_b64_tr_b16 v[38:39], v27 offset:1024
  s_xor_b32 s59, s57, 1024
  ds_read_b64_tr_b16 v[40:41], v24 offset:4096
  s_xor_b32 s73, s70, 6144
  ds_read_b64_tr_b16 v[42:43], v24 offset:5120
  s_xor_b32 s60, s57, 1536
  ds_read_b64_tr_b16 v[44:45], v25 offset:4096
  ds_read_b64_tr_b16 v[46:47], v25 offset:5120
  ds_read_b64_tr_b16 v[48:49], v26 offset:4096
  ds_read_b64_tr_b16 v[50:51], v26 offset:5120
  ds_read_b64_tr_b16 v[52:53], v27 offset:4096
  ds_read_b64_tr_b16 v[54:55], v27 offset:5120
  ds_read_b64_tr_b16 v[56:57], v24 offset:8192
  ds_read_b64_tr_b16 v[58:59], v24 offset:9216
  ds_read_b64_tr_b16 v[60:61], v25 offset:8192
  ds_read_b64_tr_b16 v[62:63], v25 offset:9216
  ds_read_b64_tr_b16 v[64:65], v26 offset:8192
  ds_read_b64_tr_b16 v[66:67], v26 offset:9216
  ds_read_b64_tr_b16 v[68:69], v27 offset:8192
  ds_read_b64_tr_b16 v[70:71], v27 offset:9216
  ds_read_b64_tr_b16 v[72:73], v24 offset:12288
  ds_read_b64_tr_b16 v[74:75], v24 offset:13312
  ds_read_b64_tr_b16 v[76:77], v25 offset:12288
  ds_read_b64_tr_b16 v[78:79], v25 offset:13312
  ds_read_b64_tr_b16 v[80:81], v26 offset:12288
  ds_read_b64_tr_b16 v[82:83], v26 offset:13312
  ds_read_b64_tr_b16 v[84:85], v27 offset:12288
  ds_read_b64_tr_b16 v[86:87], v27 offset:13312
  buffer_load_dwordx4 v[4:7], v12, s[36:39], s70 offen sc1
  buffer_load_dwordx4 v[8:11], v12, s[36:39], s70 offen offset:1024 sc1
  buffer_load_dword v22, v13, s[36:39], s57 offen sc1
  buffer_load_dwordx4 v[168:171], v12, s[36:39], s71 offen sc1
  buffer_load_dwordx4 v[172:175], v12, s[36:39], s71 offen offset:1024 sc1
  buffer_load_dword v23, v13, s[36:39], s58 offen sc1
  ds_read_b64_tr_b16 v[88:89], v24 offset:16384
  ds_read_b64_tr_b16 v[90:91], v24 offset:17408
  ds_read_b64_tr_b16 v[92:93], v25 offset:16384
  ds_read_b64_tr_b16 v[94:95], v25 offset:17408
  ds_read_b64_tr_b16 v[96:97], v26 offset:16384
  ds_read_b64_tr_b16 v[98:99], v26 offset:17408
  ds_read_b64_tr_b16 v[100:101], v27 offset:16384
  ds_read_b64_tr_b16 v[102:103], v27 offset:17408
  ds_read_b64_tr_b16 v[104:105], v24 offset:20480
  ds_read_b64_tr_b16 v[106:107], v24 offset:21504
  ds_read_b64_tr_b16 v[108:109], v25 offset:20480
  ds_read_b64_tr_b16 v[110:111], v25 offset:21504
  ds_read_b64_tr_b16 v[112:113], v26 offset:20480
  ds_read_b64_tr_b16 v[114:115], v26 offset:21504
  ds_read_b64_tr_b16 v[116:117], v27 offset:20480
  ds_read_b64_tr_b16 v[118:119], v27 offset:21504
  buffer_load_dwordx4 v[176:179], v12, s[36:39], s72 offen sc1
  buffer_load_dwordx4 v[180:183], v12, s[36:39], s72 offen offset:1024 sc1
  buffer_load_dword v192, v13, s[36:39], s59 offen sc1
  buffer_load_dwordx4 v[184:187], v12, s[36:39], s73 offen sc1
  buffer_load_dwordx4 v[188:191], v12, s[36:39], s73 offen offset:1024 sc1
  buffer_load_dword v193, v13, s[36:39], s60 offen sc1
  ds_read_b64_tr_b16 v[120:121], v24 offset:24576
  ds_read_b64_tr_b16 v[122:123], v24 offset:25600
  ds_read_b64_tr_b16 v[140:141], v25 offset:24576
  ds_read_b64_tr_b16 v[142:143], v25 offset:25600
  ds_read_b64_tr_b16 v[144:145], v26 offset:24576
  ds_read_b64_tr_b16 v[146:147], v26 offset:25600
  ds_read_b64_tr_b16 v[148:149], v27 offset:24576
  ds_read_b64_tr_b16 v[150:151], v27 offset:25600
  ds_read_b64_tr_b16 v[152:153], v24 offset:28672
  ds_read_b64_tr_b16 v[154:155], v24 offset:29696
  ds_read_b64_tr_b16 v[156:157], v25 offset:28672
  ds_read_b64_tr_b16 v[158:159], v25 offset:29696
  ds_read_b64_tr_b16 v[160:161], v26 offset:28672
  ds_read_b64_tr_b16 v[162:163], v26 offset:29696
  ds_read_b64_tr_b16 v[164:165], v27 offset:28672
  ds_read_b64_tr_b16 v[166:167], v27 offset:29696
  v_mov_b64 v[194:195], 0
  v_mov_b64 v[196:197], 0
  ds_read_u16_d16_hi v194, v21 offset:0
  ds_read_u16_d16_hi v195, v21 offset:32
  ds_read_u16_d16_hi v196, v21 offset:64
  ds_read_u16_d16_hi v197, v21 offset:96
  s_lshl3_add_u32 s53, s47, s44
  s_lshl_b32 s53, s53, 10
  s_add_u32 s53, s53, 7471104
  s_mov_b32 s68, 0xff00ff00
  s_mov_b32 s69, 0xff00ff00
  s_mov_b32 s64, 0x01010101
  s_mov_b32 s65, 0x01010101
  s_waitcnt vmcnt(11)
  v_bitop3_b32 v198, v4, v5, v6 bitop3:0x96
  s_waitcnt vmcnt(10)
  v_bitop3_b32 v199, v8, v9, v10 bitop3:0x96
  v_add3_u32 v198, v198, v7, v11
  v_add_u32 v198, v198, v199
  s_waitcnt vmcnt(9)
  v_subrev_u32 v199, s50, v22
  v_add_u32_dpp v198, v198, v198 quad_perm:[1,0,3,2] row_mask:0xf bank_mask:0xf
  v_cmp_ne_u32 vcc, v198, v199
  s_cbranch_vccnz .Lfb_o0_retry
.Lfb_o0_ok:
  s_waitcnt lgkmcnt(14)
  v_mfma_f32_16x16x32_bf16 v[124:127], v[4:7], v[14:17], v[124:127]
  v_mfma_f32_16x16x32_bf16 v[128:131], v[4:7], v[28:31], v[128:131]
  v_mfma_f32_16x16x32_bf16 v[132:135], v[4:7], v[32:35], v[132:135]
  v_mfma_f32_16x16x32_bf16 v[136:139], v[4:7], v[36:39], v[136:139]
  s_waitcnt vmcnt(8)
  v_bitop3_b32 v198, v168, v169, v170 bitop3:0x96
  s_waitcnt vmcnt(7)
  v_bitop3_b32 v199, v172, v173, v174 bitop3:0x96
  v_mfma_f32_16x16x32_bf16 v[124:127], v[8:11], v[40:43], v[124:127]
  v_add3_u32 v198, v198, v171, v175
  v_add_u32 v198, v198, v199
  v_mfma_f32_16x16x32_bf16 v[128:131], v[8:11], v[44:47], v[128:131]
  s_waitcnt vmcnt(6)
  v_subrev_u32 v199, s50, v23
  v_mfma_f32_16x16x32_bf16 v[132:135], v[8:11], v[48:51], v[132:135]
  v_add_u32_dpp v198, v198, v198 quad_perm:[1,0,3,2] row_mask:0xf bank_mask:0xf
  v_cmp_ne_u32 vcc, v198, v199
  v_mfma_f32_16x16x32_bf16 v[136:139], v[8:11], v[52:55], v[136:139]
  s_cbranch_vccnz .Lfb_o1_retry
.Lfb_o1_ok:
  v_mfma_f32_16x16x32_bf16 v[124:127], v[168:171], v[56:59], v[124:127]
  v_mfma_f32_16x16x32_bf16 v[128:131], v[168:171], v[60:63], v[128:131]
  v_mfma_f32_16x16x32_bf16 v[132:135], v[168:171], v[64:67], v[132:135]
  v_mfma_f32_16x16x32_bf16 v[136:139], v[168:171], v[68:71], v[136:139]
  s_waitcnt vmcnt(5)
  v_bitop3_b32 v198, v176, v177, v178 bitop3:0x96
  s_waitcnt vmcnt(4)
  v_bitop3_b32 v199, v180, v181, v182 bitop3:0x96
  v_mfma_f32_16x16x32_bf16 v[124:127], v[172:175], v[72:75], v[124:127]
  v_add3_u32 v198, v198, v179, v183
  v_add_u32 v198, v198, v199
  v_mfma_f32_16x16x32_bf16 v[128:131], v[172:175], v[76:79], v[128:131]
  s_waitcnt vmcnt(3)
  v_subrev_u32 v199, s50, v192
  v_mfma_f32_16x16x32_bf16 v[132:135], v[172:175], v[80:83], v[132:135]
  v_add_u32_dpp v198, v198, v198 quad_perm:[1,0,3,2] row_mask:0xf bank_mask:0xf
  v_cmp_ne_u32 vcc, v198, v199
  v_mfma_f32_16x16x32_bf16 v[136:139], v[172:175], v[84:87], v[136:139]
  s_cbranch_vccnz .Lfb_o2_retry
.Lfb_o2_ok:
  v_mfma_f32_16x16x32_bf16 v[124:127], v[176:179], v[88:91], v[124:127]
  v_mfma_f32_16x16x32_bf16 v[128:131], v[176:179], v[92:95], v[128:131]
  v_mfma_f32_16x16x32_bf16 v[132:135], v[176:179], v[96:99], v[132:135]
  v_mfma_f32_16x16x32_bf16 v[136:139], v[176:179], v[100:103], v[136:139]
  s_waitcnt vmcnt(2)
  v_bitop3_b32 v198, v184, v185, v186 bitop3:0x96
  s_waitcnt vmcnt(1)
  v_bitop3_b32 v199, v188, v189, v190 bitop3:0x96
  v_mfma_f32_16x16x32_bf16 v[124:127], v[180:183], v[104:107], v[124:127]
  v_add3_u32 v198, v198, v187, v191
  v_add_u32 v198, v198, v199
  v_mfma_f32_16x16x32_bf16 v[128:131], v[180:183], v[108:111], v[128:131]
  s_waitcnt vmcnt(0)
  v_subrev_u32 v199, s50, v193
  v_mfma_f32_16x16x32_bf16 v[132:135], v[180:183], v[112:115], v[132:135]
  v_add_u32_dpp v198, v198, v198 quad_perm:[1,0,3,2] row_mask:0xf bank_mask:0xf
  v_cmp_ne_u32 vcc, v198, v199
  v_mfma_f32_16x16x32_bf16 v[136:139], v[180:183], v[116:119], v[136:139]
  s_cbranch_vccnz .Lfb_o3_retry
.Lfb_o3_ok:
  v_mfma_f32_16x16x32_bf16 v[124:127], v[184:187], v[120:123], v[124:127]
  v_mfma_f32_16x16x32_bf16 v[128:131], v[184:187], v[140:143], v[128:131]
  v_mfma_f32_16x16x32_bf16 v[132:135], v[184:187], v[144:147], v[132:135]
  s_waitcnt lgkmcnt(12)
  v_mfma_f32_16x16x32_bf16 v[136:139], v[184:187], v[148:151], v[136:139]
  s_waitcnt lgkmcnt(10)
  v_mfma_f32_16x16x32_bf16 v[124:127], v[188:191], v[152:155], v[124:127]
  s_waitcnt lgkmcnt(8)
  v_mfma_f32_16x16x32_bf16 v[128:131], v[188:191], v[156:159], v[128:131]
  s_waitcnt lgkmcnt(6)
  v_mfma_f32_16x16x32_bf16 v[132:135], v[188:191], v[160:163], v[132:135]
  s_waitcnt lgkmcnt(4)
  v_mfma_f32_16x16x32_bf16 v[136:139], v[188:191], v[164:167], v[136:139]
  s_nop 3
  ds_write2st64_b64 v18, v[126:127], v[130:131] offset0:0 offset1:1
  s_nop 2
  ds_write2st64_b64 v18, v[134:135], v[138:139] offset0:2 offset1:3
  s_waitcnt lgkmcnt(0)
  s_barrier
  ds_read_b64 v[4:5], v19 offset:0
  ds_read_b64 v[6:7], v19 offset:512
  ds_read_b64 v[8:9], v19 offset:1024
  ds_read_b64 v[10:11], v19 offset:1536
  s_waitcnt lgkmcnt(3)
  v_pk_add_f32 v[124:125], v[124:125], v[4:5]
  s_waitcnt lgkmcnt(2)
  v_pk_add_f32 v[128:129], v[128:129], v[6:7]
  s_waitcnt lgkmcnt(1)
  v_pk_add_f32 v[132:133], v[132:133], v[8:9]
  s_waitcnt lgkmcnt(0)
  v_pk_add_f32 v[136:137], v[136:137], v[10:11]
; LN2 partial statistics of this 64-column slice: lane (c, g) ends with local row 2 g + (c >> 3)
  v_pk_mul_f32 v[14:15], v[124:125], v[124:125]
  v_pk_mul_f32 v[16:17], v[124:125], v[194:195] op_sel_hi:[1,0]
  v_pk_add_f32 v[12:13], v[124:125], v[128:129]
  v_pk_fma_f32 v[14:15], v[128:129], v[128:129], v[14:15]
  v_pk_fma_f32 v[16:17], v[194:195], v[128:129], v[16:17] op_sel:[1,0,0] op_sel_hi:[1,1,1]
  v_pk_fma_f32 v[14:15], v[132:133], v[132:133], v[14:15]
  v_pk_fma_f32 v[16:17], v[196:197], v[132:133], v[16:17] op_sel:[0,0,0] op_sel_hi:[0,1,1]
  v_pk_add_f32 v[12:13], v[12:13], v[132:133]
  v_pk_fma_f32 v[14:15], v[136:137], v[136:137], v[14:15]
  v_pk_fma_f32 v[16:17], v[196:197], v[136:137], v[16:17] op_sel:[1,0,0] op_sel_hi:[1,1,1]
  v_pk_add_f32 v[12:13], v[12:13], v[136:137]
  v_cndmask_b32_e64 v5, v15, v14, s[68:69]
  v_cndmask_b32_e64 v6, v17, v16, s[68:69]
  v_cndmask_b32_e64 v4, v13, v12, s[68:69]
  v_cndmask_b32_e64 v25, v14, v15, s[68:69]
  v_cndmask_b32_e64 v26, v16, v17, s[68:69]
  v_cndmask_b32_e64 v24, v12, v13, s[68:69]
  v_add_f32_dpp v25, v5, v25 row_ror:8 row_mask:0xf bank_mask:0xf
  v_add_f32_dpp v26, v6, v26 row_ror:8 row_mask:0xf bank_mask:0xf
  v_add_f32_dpp v24, v4, v24 row_ror:8 row_mask:0xf bank_mask:0xf
  v_add_f32_dpp v25, v25, v25 quad_perm:[1,0,3,2] row_mask:0xf bank_mask:0xf
  v_add_f32_dpp v26, v26, v26 quad_perm:[1,0,3,2] row_mask:0xf bank_mask:0xf
  v_add_f32_dpp v24, v24, v24 quad_perm:[1,0,3,2] row_mask:0xf bank_mask:0xf
  v_add_f32_dpp v25, v25, v25 quad_perm:[2,3,0,1] row_mask:0xf bank_mask:0xf
  v_add_f32_dpp v26, v26, v26 quad_perm:[2,3,0,1] row_mask:0xf bank_mask:0xf
  v_add_f32_dpp v24, v24, v24 quad_perm:[2,3,0,1] row_mask:0xf bank_mask:0xf
  v_add_f32_dpp v25, v25, v25 row_half_mirror row_mask:0xf bank_mask:0xf
  v_add_f32_dpp v26, v26, v26 row_half_mirror row_mask:0xf bank_mask:0xf
  v_add_f32_dpp v24, v24, v24 row_half_mirror row_mask:0xf bank_mask:0xf
  v_add3_u32 v27, v25, v26, s50
  s_mov_b64 exec, s[64:65]
  v_add_u32 v27, v27, v24
  buffer_store_dwordx4 v[24:27], v20, s[36:39], s53 offen sc1
  s_mov_b64 exec, -1
; while the statistics travel: transpose h so that lane (rr, s8) holds columns 8 s8 .. 8 s8 + 7 of local row rr
  v_add_u32 v33, 160256, v2
  ds_read_b128 v[34:37], v33 offset:0
  v_and_b32 v33, 15, v1
  s_mul_i32 s56, s44, 2304
  s_add_u32 s56, s56, 65536
  v_lshrrev_b32 v38, 4, v1
  v_mul_u32_u24 v28, 576, v38
  v_lshl_add_u32 v28, v33, 2, v28
  v_add_u32 v28, s56, v28
  ds_write2_b32 v28, v124, v125 offset0:0 offset1:72
  ds_write2_b32 v28, v128, v129 offset0:16 offset1:88
  ds_write2_b32 v28, v132, v133 offset0:32 offset1:104
  ds_write2_b32 v28, v136, v137 offset0:48 offset1:120
  v_lshrrev_b32 v33, 3, v1
  v_and_b32 v32, 0x70, v2
  v_mul_u32_u24 v29, 288, v33
  v_lshl_add_u32 v29, v32, 1, v29
  v_add_u32 v29, s56, v29
  v_lshlrev_b32 v21, 4, v33
  v_lshl_add_u32 v21, v32, 3, v21
  v_and_b32 v33, 1, v33
  v_lshl_add_u32 v33, v38, 2, v33
  s_lshl3_add_u32 s56, s45, s46
  s_lshl_b32 s56, s56, 1
  v_add_u32 v33, s56, v33
  v_sub_u32 v30, 63, v33
  v_lshl_add_u32 v31, v33, 10, v32
  s_waitcnt lgkmcnt(4)
  v_lshlrev_b32 v18, 16, v34
  v_and_b32 v38, 0xffff0000, v34
  v_add_f32 v18, v18, v38
  v_lshlrev_b32 v33, 16, v35
  v_and_b32 v38, 0xffff0000, v35
  v_add_f32 v18, v18, v33
  v_add_f32 v18, v18, v38
  v_lshlrev_b32 v33, 16, v36
  v_and_b32 v38, 0xffff0000, v36
  v_add_f32 v18, v18, v33
  v_add_f32 v18, v18, v38
  v_lshlrev_b32 v33, 16, v37
  v_and_b32 v38, 0xffff0000, v37
  v_add_f32 v18, v18, v33
  v_add_f32 v18, v18, v38
  s_nop 1
  v_add_f32_dpp v18, v18, v18 quad_perm:[1,0,3,2] row_mask:0xf bank_mask:0xf
  s_lshl_b32 s62, s48, 5
  s_add_u32 s62, s62, s47
  v_add_f32_dpp v18, v18, v18 quad_perm:[2,3,0,1] row_mask:0xf bank_mask:0xf
  s_lshl_b32 s62, s62, 8
  s_add_u32 s62, s62, 7733248
  v_add_f32_dpp v18, v18, v18 row_half_mirror row_mask:0xf bank_mask:0xf
  s_lshl_b32 s52, s47, 16
  s_lshl_b32 s55, s48, 7
  v_add_f32_dpp v18, v18, v18 row_ror:8 row_mask:0xf bank_mask:0xf
  s_add_u32 s52, s52, s55
  v_mov_b32 v33, v18
  s_nop 1
  v_permlane16_swap_b32 v18, v33
  v_add_f32 v18, v18, v33
  v_mov_b32 v33, v18
  s_nop 1
  v_permlane32_swap_b32 v18, v33
  v_add_f32 v18, v18, v33
  ds_read_b128 v[40:43], v29 offset:0
  ds_read_b128 v[44:47], v29 offset:16
  v_mov_b32 v39, 162304
  s_sub_u32 s55, 63, s47
  s_movk_i32 s56, 0xffc0
  v_mov_b32 v71, s55
  s_waitcnt lgkmcnt(0)
.Lfb_h3:
  buffer_load_dwordx4 v[48:51], v21, s[36:39], s53 offen sc1
  s_waitcnt vmcnt(0)
  v_add3_u32 v70, v48, v49, v50
  v_subrev_u32 v51, s50, v51
  v_cmp_ne_u32 vcc, v70, v51
  v_add_f32_dpp v48, v48, v48 quad_perm:[1,0,3,2] row_mask:0xf bank_mask:0xf
  v_add_f32_dpp v49, v49, v49 quad_perm:[1,0,3,2] row_mask:0xf bank_mask:0xf
  v_add_f32_dpp v50, v50, v50 quad_perm:[1,0,3,2] row_mask:0xf bank_mask:0xf
  v_add_f32_dpp v48, v48, v48 quad_perm:[2,3,0,1] row_mask:0xf bank_mask:0xf
  v_add_f32_dpp v49, v49, v49 quad_perm:[2,3,0,1] row_mask:0xf bank_mask:0xf
  v_add_f32_dpp v50, v50, v50 quad_perm:[2,3,0,1] row_mask:0xf bank_mask:0xf
  s_cbranch_vccnz .Lfb_h3_retry
.Lfb_h3_ok:
  v_add_f32_dpp v48, v48, v48 row_half_mirror row_mask:0xf bank_mask:0xf
  v_add_f32_dpp v49, v49, v49 row_half_mirror row_mask:0xf bank_mask:0xf
  v_add_f32_dpp v50, v50, v50 row_half_mirror row_mask:0xf bank_mask:0xf
  v_mul_f32 v68, 0x3b000000, v48
  v_mul_f32 v49, 0x3b000000, v49
  v_fma_f32 v49, -v68, v68, v49
  v_max_f32 v49, 0, v49
  v_add_f32 v49, 0x3727c5ac, v49
  v_rsq_f32 v69, v49
  v_fma_f32 v50, -v68, v18, v50
  v_mul_f32 v50, v50, v69
  s_brev_b32 s57, 1
  v_ashrrev_i32 v70, 31, v50
  v_bitop3_b32 v70, v50, v70, s57 bitop3:0x1e
  s_mov_b64 exec, s[64:65]
  v_and_or_b32 v51, v70, s56, v30
  ds_max_u32 v39, v51
  s_mov_b64 exec, -1
  v_and_or_b32 v30, v70, s56, v30
  v_and_or_b32 v62, v70, s56, v71
  v_pk_add_f32 v[52:53], v[40:41], v[68:69] op_sel_hi:[1,0] neg_lo:[0,1] neg_hi:[0,1]
  v_pk_add_f32 v[54:55], v[42:43], v[68:69] op_sel_hi:[1,0] neg_lo:[0,1] neg_hi:[0,1]
  v_pk_add_f32 v[56:57], v[44:45], v[68:69] op_sel_hi:[1,0] neg_lo:[0,1] neg_hi:[0,1]
  v_pk_add_f32 v[58:59], v[46:47], v[68:69] op_sel_hi:[1,0] neg_lo:[0,1] neg_hi:[0,1]
  v_pk_mul_f32 v[52:53], v[68:69], v[52:53] op_sel:[1,0] op_sel_hi:[1,1]
  v_pk_mul_f32 v[54:55], v[68:69], v[54:55] op_sel:[1,0] op_sel_hi:[1,1]
  v_pk_mul_f32 v[56:57], v[68:69], v[56:57] op_sel:[1,0] op_sel_hi:[1,1]
  v_pk_mul_f32 v[58:59], v[68:69], v[58:59] op_sel:[1,0] op_sel_hi:[1,1]
  s_waitcnt lgkmcnt(0)
  s_barrier
  ds_read_b32 v72, v39
  v_cvt_pk_bf16_f32 v60, v52, v53
  v_cvt_pk_bf16_f32 v61, v54, v55
  v_cvt_pk_bf16_f32 v64, v56, v57
  v_cvt_pk_bf16_f32 v65, v58, v59
  v_mov_b32 v66, v62
  v_add_u32 v70, s50, v62
  v_add3_u32 v63, v60, v61, v70
  v_add3_u32 v67, v64, v65, v70
  s_waitcnt lgkmcnt(0)
  v_cmp_eq_u32 vcc, v72, v30
  s_cbranch_vccz .Lfb_plain
  s_mov_b64 exec, vcc
  buffer_store_dwordx4 v[60:63], v32, s[36:39], s62 offen sc1
  buffer_store_dwordx4 v[64:67], v32, s[36:39], s62 offen offset:128 sc1
  v_readfirstlane_b32 s49, v50
  v_readfirstlane_b32 s63, v31
  s_mov_b64 exec, -1
; ==== routed FFN: the wave that holds the candidate row
  v_pk_add_f32 v[40:41], v[40:41], v[52:53]
  v_pk_add_f32 v[42:43], v[42:43], v[54:55]
  v_pk_add_f32 v[44:45], v[44:45], v[56:57]
  v_pk_add_f32 v[46:47], v[46:47], v[58:59]
  v_cvt_pk_bf16_f32 v74, v40, v41
  v_cvt_pk_bf16_f32 v75, v42, v43
  v_cvt_pk_bf16_f32 v76, v44, v45
  v_cvt_pk_bf16_f32 v77, v46, v47
  v_add_u32 v70, 162432, v32
  s_mov_b64 exec, vcc
  ds_write_b128 v70, v[74:77]
  s_not_b64 exec, vcc
  buffer_store_dwordx4 v[74:77], v31, s[32:35], s52 offen sc1
  s_mov_b64 exec, -1
  v_bfe_u32 v148, v1, 2, 2
  v_and_b32 v151, 48, v1
  v_lshlrev_b32 v149, 6, v151
  v_lshl_or_b32 v149, v148, 7, v149
  v_and_b32 v148, 3, v1
  v_lshl_or_b32 v149, v148, 3, v149
  v_and_b32 v148, 24, v1
  v_lshl_or_b32 v149, v148, 2, v149
  v_add_u32 v72, 122880, v149
  v_xor_b32 v73, 32, v72
  v_xor_b32 v114, 64, v72
  v_xor_b32 v115, 96, v72
  ds_read_b64_tr_b16 v[4:5], v72 offset:0
  v_and_b32 v148, 0x70, v2
  ds_read_b64_tr_b16 v[6:7], v72 offset:512
  v_and_or_b32 v148, v1, 8, v148
  ds_read_b64_tr_b16 v[8:9], v73 offset:0
  v_lshl_add_u32 v150, v151, 3, v148
  ds_read_b64_tr_b16 v[10:11], v73 offset:512
  v_add_u32 v150, 163072, v150
  ds_read_b64_tr_b16 v[12:13], v114 offset:0
  v_add_u32 v151, 163072, v151
  ds_read_b64_tr_b16 v[14:15], v114 offset:512
  v_lshlrev_b32 v154, 3, v1
  ds_read_b64_tr_b16 v[16:17], v115 offset:0
  v_and_b32 v148, 0x78, v154
  ds_read_b64_tr_b16 v[18:19], v115 offset:512
  v_add_u32 v153, s63, v148
  ds_read_b64_tr_b16 v[20:21], v72 offset:4096
  v_add_u32 v148, 162432, v148
  ds_read_b64_tr_b16 v[22:23], v72 offset:4608
  ds_read_b64 v[156:157], v148
  ds_read_b64_tr_b16 v[24:25], v73 offset:4096
  s_lshl_b32 s55, s48, 13
  ds_read_b64_tr_b16 v[26:27], v73 offset:4608
  s_add_u32 s55, s55, 7733248
  ds_read_b64_tr_b16 v[28:29], v114 offset:4096
  s_add_u32 s56, s55, 4096
  ds_read_b64_tr_b16 v[30:31], v114 offset:4608
  s_lshl3_add_u32 s59, s47, s48
  ds_read_b64_tr_b16 v[32:33], v115 offset:4096
  s_lshl_b32 s59, s59, 9
  ds_read_b64_tr_b16 v[34:35], v115 offset:4608
  s_add_u32 s59, s59, 7798784
  ds_read_b64_tr_b16 v[36:37], v72 offset:8192
  s_mov_b32 s68, 0xffff0000
  ds_read_b64_tr_b16 v[38:39], v72 offset:8704
  s_mov_b32 s69, 0xffff0000
  ds_read_b64_tr_b16 v[40:41], v73 offset:8192
  s_mov_b32 s70, 0
  ds_read_b64_tr_b16 v[42:43], v73 offset:8704
  s_mov_b32 s71, -1
  ds_read_b64_tr_b16 v[44:45], v114 offset:8192
  s_movk_i32 s60, 0xff00
  ds_read_b64_tr_b16 v[46:47], v114 offset:8704
  v_mov_b32 v149, s61
  ds_read_b64_tr_b16 v[48:49], v115 offset:8192
  ds_read_b64_tr_b16 v[50:51], v115 offset:8704
  ds_read_b64_tr_b16 v[52:53], v72 offset:12288
  ds_read_b64_tr_b16 v[54:55], v72 offset:12800
  ds_read_b64_tr_b16 v[56:57], v73 offset:12288
  ds_read_b64_tr_b16 v[58:59], v73 offset:12800
  ds_read_b64_tr_b16 v[68:69], v114 offset:12288
  ds_read_b64_tr_b16 v[70:71], v114 offset:12800
  ds_read_b64_tr_b16 v[78:79], v115 offset:12288
  ds_read_b64_tr_b16 v[80:81], v115 offset:12800
  ds_read_b64_tr_b16 v[82:83], v72 offset:16384
  ds_read_b64_tr_b16 v[84:85], v72 offset:16896
  ds_read_b64_tr_b16 v[86:87], v73 offset:16384
  ds_read_b64_tr_b16 v[88:89], v73 offset:16896
  ds_read_b64_tr_b16 v[90:91], v114 offset:16384
  ds_read_b64_tr_b16 v[92:93], v114 offset:16896
  ds_read_b64_tr_b16 v[94:95], v115 offset:16384
  ds_read_b64_tr_b16 v[96:97], v115 offset:16896
  ds_read_b64_tr_b16 v[98:99], v72 offset:20480
  ds_read_b64_tr_b16 v[100:101], v72 offset:20992
  ds_read_b64_tr_b16 v[102:103], v73 offset:20480
  ds_read_b64_tr_b16 v[104:105], v73 offset:20992
  ds_read_b64_tr_b16 v[106:107], v114 offset:20480
  ds_read_b64_tr_b16 v[108:109], v114 offset:20992
  ds_read_b64_tr_b16 v[110:111], v115 offset:20480
  ds_read_b64_tr_b16 v[112:113], v115 offset:20992
  s_waitcnt lgkmcnt(0)
  s_sleep 1
.Lfb_h4:
  buffer_load_dwordx4 v[116:119], v2, s[36:39], s55 offen offset:0 sc1
  buffer_load_dwordx4 v[120:123], v2, s[36:39], s55 offen offset:1024 sc1
  buffer_load_dwordx4 v[124:127], v2, s[36:39], s55 offen offset:2048 sc1
  buffer_load_dwordx4 v[128:131], v2, s[36:39], s55 offen offset:3072 sc1
  buffer_load_dwordx4 v[132:135], v2, s[36:39], s56 offen offset:0 sc1
  buffer_load_dwordx4 v[136:139], v2, s[36:39], s56 offen offset:1024 sc1
  buffer_load_dwordx4 v[140:143], v2, s[36:39], s56 offen offset:2048 sc1
  buffer_load_dwordx4 v[144:147], v2, s[36:39], s56 offen offset:3072 sc1
  s_waitcnt vmcnt(5)
  v_max3_u32 v155, v118, v122, v126
  s_waitcnt vmcnt(2)
  v_max3_u32 v148, v130, v134, v138
  s_waitcnt vmcnt(0)
  v_max3_u32 v155, v155, v142, v146
  v_max_u32 v155, v155, v148
  v_mov_b32 v148, v155
  v_add3_u32 v118, v116, v117, v118
  v_add3_u32 v122, v120, v121, v122
  v_permlane16_swap_b32 v155, v148
  v_max_u32 v155, v155, v148
  v_mov_b32 v148, v155
  v_add3_u32 v126, v124, v125, v126
  v_add3_u32 v130, v128, v129, v130
  v_permlane32_swap_b32 v155, v148
  v_max_u32 v155, v155, v148
  v_add3_u32 v134, v132, v133, v134
  v_readfirstlane_b32 s57, v155
  s_andn2_b32 s57, 31, s57
  s_and_b32 s58, s57, 28
  s_set_gpr_idx_on s58, gpr_idx(SRC0)
  v_mov_b32 v158, v116
  v_mov_b32 v159, v117
  s_set_gpr_idx_off
  s_and_b32 s58, s57, 3
  ds_write_b64 v150, v[158:159]
  v_lshl_add_u32 v152, s58, 7, v151
  ds_read_b128 v[160:163], v152 offset:0
  ds_read_b128 v[164:167], v152 offset:64
  v_add3_u32 v138, v136, v137, v138
  v_add3_u32 v142, v140, v141, v142
  v_add3_u32 v146, v144, v145, v146
  v_sub_u32 v118, v119, v118
  v_sub_u32 v122, v123, v122
  v_sub_u32 v126, v127, v126
  v_sub_u32 v130, v131, v130
  v_sub_u32 v134, v135, v134
  v_sub_u32 v138, v139, v138
  v_sub_u32 v142, v143, v142
  v_sub_u32 v146, v147, v146
  v_bitop3_b32 v118, v118, v122, s50 bitop3:0x7e
  v_bitop3_b32 v126, v126, v130, s50 bitop3:0x7e
  v_bitop3_b32 v134, v134, v138, s50 bitop3:0x7e
  v_bitop3_b32 v142, v142, v146, s50 bitop3:0x7e
  v_or3_b32 v118, v118, v126, v134
  v_or_b32 v118, v118, v142
  v_cmp_ne_u32 vcc, 0, v118
  s_cbranch_vccnz .Lfb_h4_retry
.Lfb_h4_ok:
  s_cmp_ge_u32 s47, 28
  s_cbranch_scc1 .Lfb_noffn
  s_waitcnt lgkmcnt(1)
  v_mfma_f32_16x16x32_bf16 v[168:171], v[160:163], v[4:7], 0
  v_mfma_f32_16x16x32_bf16 v[172:175], v[160:163], v[8:11], 0
  v_mfma_f32_16x16x32_bf16 v[176:179], v[160:163], v[12:15], 0
  v_mfma_f32_16x16x32_bf16 v[180:183], v[160:163], v[16:19], 0
  v_mfma_f32_16x16x32_bf16 v[184:187], v[160:163], v[36:39], 0
  v_mfma_f32_16x16x32_bf16 v[188:191], v[160:163], v[40:43], 0
  v_mfma_f32_16x16x32_bf16 v[192:195], v[160:163], v[44:47], 0
  v_mfma_f32_16x16x32_bf16 v[196:199], v[160:163], v[48:51], 0
  s_waitcnt lgkmcnt(0)
  v_mfma_f32_16x16x32_bf16 v[168:171], v[164:167], v[20:23], v[168:171]
  v_mfma_f32_16x16x32_bf16 v[172:175], v[164:167], v[24:27], v[172:175]
  v_mfma_f32_16x16x32_bf16 v[176:179], v[164:167], v[28:31], v[176:179]
  v_mfma_f32_16x16x32_bf16 v[180:183], v[164:167], v[32:35], v[180:183]
  v_mfma_f32_16x16x32_bf16 v[184:187], v[164:167], v[52:55], v[184:187]
  v_mfma_f32_16x16x32_bf16 v[188:191], v[164:167], v[56:59], v[188:191]
  v_mfma_f32_16x16x32_bf16 v[192:195], v[164:167], v[68:71], v[192:195]
  v_mfma_f32_16x16x32_bf16 v[196:199], v[164:167], v[78:81], v[196:199]
  s_nop 1
  v_cndmask_b32_e64 v168, v168, v172, s[68:69]
  s_nop 0
  v_cndmask_b32_e64 v176, v176, v180, s[68:69]
  v_cndmask_b32_e64 v158, v168, v176, s[70:71]
  v_and_or_b32 v158, v158, s60, v149
  v_cndmask_b32_e64 v184, v184, v188, s[68:69]
  v_cndmask_b32_e64 v192, v192, v196, s[68:69]
  v_cndmask_b32_e64 v159, v184, v192, s[70:71]
  v_and_or_b32 v159, v159, s60, v149
  buffer_store_dwordx2 v[158:159], v154, s[36:39], s59 offen sc1
  s_lshl_b32 s55, s47, 12
  s_add_u32 s55, s55, 7798784
  s_mul_i32 s59, s48, 28
  s_add_u32 s59, s59, s47
  s_lshl_b32 s59, s59, 8
  s_add_u32 s59, s59, 7929856
  v_lshlrev_b32 v150, 1, v1
  v_add_u32 v150, 163072, v150
  s_sleep 5
.Lfb_h5:
  buffer_load_dwordx2 v[116:117], v154, s[36:39], s55 offen offset:0 sc1
  buffer_load_dwordx2 v[118:119], v154, s[36:39], s55 offen offset:512 sc1
  buffer_load_dwordx2 v[120:121], v154, s[36:39], s55 offen offset:1024 sc1
  buffer_load_dwordx2 v[122:123], v154, s[36:39], s55 offen offset:1536 sc1
  buffer_load_dwordx2 v[124:125], v154, s[36:39], s55 offen offset:2048 sc1
  buffer_load_dwordx2 v[126:127], v154, s[36:39], s55 offen offset:2560 sc1
  buffer_load_dwordx2 v[128:129], v154, s[36:39], s55 offen offset:3072 sc1
  buffer_load_dwordx2 v[130:131], v154, s[36:39], s55 offen offset:3584 sc1
  s_waitcnt vmcnt(7)
  v_xor_b32 v116, v149, v116
  v_xor_b32 v117, v149, v117
  s_waitcnt vmcnt(6)
  v_xor_b32 v118, v149, v118
  v_xor_b32 v119, v149, v119
  v_pk_add_f32 v[132:133], v[116:117], v[118:119]
  s_waitcnt vmcnt(5)
  v_xor_b32 v120, v149, v120
  v_xor_b32 v121, v149, v121
  s_waitcnt vmcnt(4)
  v_xor_b32 v122, v149, v122
  v_xor_b32 v123, v149, v123
  v_pk_add_f32 v[134:135], v[120:121], v[122:123]
  s_waitcnt vmcnt(3)
  v_xor_b32 v124, v149, v124
  v_xor_b32 v125, v149, v125
  s_waitcnt vmcnt(2)
  v_xor_b32 v126, v149, v126
  v_xor_b32 v127, v149, v127
  v_pk_add_f32 v[136:137], v[124:125], v[126:127]
  s_waitcnt vmcnt(1)
  v_xor_b32 v128, v149, v128
  v_xor_b32 v129, v149, v129
  s_waitcnt vmcnt(0)
  v_xor_b32 v130, v149, v130
  v_xor_b32 v131, v149, v131
  v_pk_add_f32 v[138:139], v[128:129], v[130:131]
  v_pk_add_f32 v[132:133], v[132:133], v[134:135]
  v_pk_add_f32 v[136:137], v[136:137], v[138:139]
  v_pk_add_f32 v[132:133], v[132:133], v[136:137]
  v_mul_f32 v148, 0xbfb8aa3b, v132
  v_exp_f32 v148, v148
  v_mul_f32 v132, v132, v133
  v_add_f32 v148, 1.0, v148
  v_rcp_f32 v148, v148
  s_nop 0
  v_mul_f32 v148, v148, v132
  v_cvt_pk_bf16_f32 v148, v148, v148
  ds_write_b16 v150, v148
  ds_read_b128 v[160:163], v151 offset:0
  ds_read_b128 v[164:167], v151 offset:64
  v_or3_b32 v140, v116, v117, v118
  v_or3_b32 v141, v119, v120, v121
  v_or3_b32 v142, v122, v123, v124
  v_or3_b32 v143, v125, v126, v127
  v_or3_b32 v144, v128, v129, v130
  v_or3_b32 v145, v131, v140, v141
  v_or3_b32 v146, v142, v143, v144
  v_bitop3_b32 v147, v145, v146, s60 bitop3:0x54
  v_cmp_ne_u32 vcc, 0, v147
  s_cbranch_vccnz .Lfb_h5_retry
.Lfb_h5_ok:
  s_waitcnt lgkmcnt(1)
  v_mfma_f32_16x16x32_bf16 v[168:171], v[160:163], v[82:85], 0
  v_mfma_f32_16x16x32_bf16 v[172:175], v[160:163], v[86:89], 0
  v_mfma_f32_16x16x32_bf16 v[176:179], v[160:163], v[90:93], 0
  v_mfma_f32_16x16x32_bf16 v[180:183], v[160:163], v[94:97], 0
  s_waitcnt lgkmcnt(0)
  v_mfma_f32_16x16x32_bf16 v[168:171], v[164:167], v[98:101], v[168:171]
  v_mfma_f32_16x16x32_bf16 v[172:175], v[164:167], v[102:105], v[172:175]
  v_mfma_f32_16x16x32_bf16 v[176:179], v[164:167], v[106:109], v[176:179]
  v_mfma_f32_16x16x32_bf16 v[180:183], v[164:167], v[110:113], v[180:183]
  s_nop 5
  v_cndmask_b32_e64 v168, v168, v172, s[68:69]
  s_nop 0
  v_cndmask_b32_e64 v176, v176, v180, s[68:69]
  v_cndmask_b32_e64 v158, v168, v176, s[70:71]
  v_and_or_b32 v158, v158, s60, v149
  buffer_store_dword v158, v3, s[36:39], s59 offen sc1
.Lfb_noffn:
  s_waitcnt lgkmcnt(0)
  s_cmp_eq_u32 s57, s47
  s_cbranch_scc0 .Lfb_lose
  v_mov_b32 v160, 0xbfb8aa3b
  v_mul_f32 v160, s49, v160
  v_exp_f32 v160, v160
  s_mul_i32 s55, s48, 7168
  v_add_f32 v160, 1.0, v160
  v_rcp_f32 v160, v160
  s_add_u32 s55, s55, 7929856
  s_add_u32 s56, s55, 4096
  v_lshlrev_b32 v164, 16, v156
  v_and_b32 v165, 0xffff0000, v156
  v_lshlrev_b32 v166, 16, v157
  v_and_b32 v167, 0xffff0000, v157
  s_sleep 5
.Lfb_h6:
  buffer_load_dwordx4 v[116:119], v2, s[36:39], s55 offen offset:0 sc1
  buffer_load_dwordx4 v[120:123], v2, s[36:39], s55 offen offset:1024 sc1
  buffer_load_dwordx4 v[124:127], v2, s[36:39], s55 offen offset:2048 sc1
  buffer_load_dwordx4 v[128:131], v2, s[36:39], s55 offen offset:3072 sc1
  buffer_load_dwordx4 v[132:135], v2, s[36:39], s56 offen offset:0 sc1
  buffer_load_dwordx4 v[136:139], v2, s[36:39], s56 offen offset:1024 sc1
  buffer_load_dwordx4 v[140:143], v2, s[36:39], s56 offen offset:2048 sc1
  s_waitcnt vmcnt(6)
  v_xor_b32 v116, v149, v116
  v_xor_b32 v117, v149, v117
  v_xor_b32 v118, v149, v118
  v_xor_b32 v119, v149, v119
  v_or3_b32 v184, v116, v117, v118
  v_mov_b32 v185, v119
  s_waitcnt vmcnt(5)
  v_xor_b32 v120, v149, v120
  v_xor_b32 v121, v149, v121
  v_xor_b32 v122, v149, v122
  v_xor_b32 v123, v149, v123
  v_or3_b32 v184, v184, v120, v121
  v_or3_b32 v185, v185, v122, v123
  v_pk_add_f32 v[168:169], v[116:117], v[120:121]
  v_pk_add_f32 v[170:171], v[118:119], v[122:123]
  s_waitcnt vmcnt(4)
  v_xor_b32 v124, v149, v124
  v_xor_b32 v125, v149, v125
  v_xor_b32 v126, v149, v126
  v_xor_b32 v127, v149, v127
  v_or3_b32 v184, v184, v124, v125
  v_or3_b32 v185, v185, v126, v127
  v_pk_add_f32 v[168:169], v[168:169], v[124:125]
  v_pk_add_f32 v[170:171], v[170:171], v[126:127]
  s_waitcnt vmcnt(3)
  v_xor_b32 v128, v149, v128
  v_xor_b32 v129, v149, v129
  v_xor_b32 v130, v149, v130
  v_xor_b32 v131, v149, v131
  v_or3_b32 v184, v184, v128, v129
  v_or3_b32 v185, v185, v130, v131
  v_pk_add_f32 v[168:169], v[168:169], v[128:129]
  v_pk_add_f32 v[170:171], v[170:171], v[130:131]
  s_waitcnt vmcnt(2)
  v_xor_b32 v132, v149, v132
  v_xor_b32 v133, v149, v133
  v_xor_b32 v134, v149, v134
  v_xor_b32 v135, v149, v135
  v_or3_b32 v184, v184, v132, v133
  v_or3_b32 v185, v185, v134, v135
  v_pk_add_f32 v[168:169], v[168:169], v[132:133]
  v_pk_add_f32 v[170:171], v[170:171], v[134:135]
  s_waitcnt vmcnt(1)
  v_xor_b32 v136, v149, v136
  v_xor_b32 v137, v149, v137
  v_xor_b32 v138, v149, v138
  v_xor_b32 v139, v149, v139
  v_or3_b32 v184, v184, v136, v137
  v_or3_b32 v185, v185, v138, v139
  v_pk_add_f32 v[168:169], v[168:169], v[136:137]
  v_pk_add_f32 v[170:171], v[170:171], v[138:139]
  s_waitcnt vmcnt(0)
  v_xor_b32 v140, v149, v140
  v_xor_b32 v141, v149, v141
  v_xor_b32 v142, v149, v142
  v_xor_b32 v143, v149, v143
  v_or3_b32 v184, v184, v140, v141
  v_or3_b32 v185, v185, v142, v143
  v_bitop3_b32 v186, v184, v185, s60 bitop3:0x54
  v_cmp_ne_u32 vcc, 0, v186
  v_pk_add_f32 v[168:169], v[168:169], v[140:141]
  v_pk_add_f32 v[170:171], v[170:171], v[142:143]
  s_cbranch_vccnz .Lfb_h6_retry
.Lfb_h6_ok:
  v_permlane16_swap_b32 v168, v169
  v_permlane16_swap_b32 v170, v171
  v_add_f32 v168, v168, v169
  v_add_f32 v170, v170, v171
  v_cndmask_b32_e64 v164, v164, v165, s[68:69]
  v_cndmask_b32_e64 v166, v166, v167, s[68:69]
  v_permlane32_swap_b32 v168, v170
  v_cndmask_b32_e64 v164, v164, v166, s[70:71]
  v_add_f32 v168, v168, v170
  v_lshrrev_b32 v148, 4, v1
  v_fma_f32 v168, v168, v160, v164
  v_lshl_add_u32 v153, v148, 1, v153
  v_cvt_pk_bf16_f32 v168, v168, v168
  buffer_store_short v168, v153, s[32:35], s52 offen sc1
  s_cmp_eq_u32 s48, 0
  s_cbranch_scc0 .Lfb_exit
  v_mov_b32 v148, s51
  s_mov_b64 exec, 1
  buffer_store_dword v148, off, s[36:39], 0 sc1
.Lfb_exit:
  s_endpgm
.Lfb_n1_retry:
  s_sub_u32 s54, s54, 1
  s_cbranch_scc1 .Lfb_n1_give
  s_sleep 1
  buffer_load_dwordx4 v[88:91], v2, s[36:39], s70 offen sc1
  s_mov_b64 exec, s[66:67]
  buffer_load_dwordx4 v[64:67], v4, s[36:39], s70 offen offset:1024 sc1
  buffer_load_dwordx4 v[68:71], v4, s[36:39], s70 offen offset:1536 sc1
  s_mov_b64 exec, -1
  buffer_load_dword v28, v3, s[36:39], s70 offen offset:2048 sc1
  s_waitcnt vmcnt(0)
  v_bitop3_b32 v229, v88, v89, v90 bitop3:0x96
  v_bitop3_b32 v230, v64, v65, v66 bitop3:0x96
  v_bitop3_b32 v231, v68, v69, v70 bitop3:0x96
  v_add3_u32 v229, v229, v230, v231
  v_bitop3_b32 v230, v91, v67, v71 bitop3:0x96
  v_add3_u32 v229, v229, v230, s50
  v_cmp_ne_u32 vcc, v229, v28
  s_cbranch_vccnz .Lfb_n1_retry
  s_branch .Lfb_n1_ok
.Lfb_n1_give:
  s_mov_b32 s54, 0
  s_branch .Lfb_n1_ok
.Lfb_n2_retry:
  s_sub_u32 s54, s54, 1
  s_cbranch_scc1 .Lfb_n2_give
  s_sleep 1
  buffer_load_dwordx4 v[92:95], v2, s[36:39], s71 offen sc1
  s_mov_b64 exec, s[66:67]
  buffer_load_dwordx4 v[72:75], v4, s[36:39], s71 offen offset:1024 sc1
  buffer_load_dwordx4 v[76:79], v4, s[36:39], s71 offen offset:1536 sc1
  s_mov_b64 exec, -1
  buffer_load_dword v29, v3, s[36:39], s71 offen offset:2048 sc1
  s_waitcnt vmcnt(0)
  v_bitop3_b32 v229, v92, v93, v94 bitop3:0x96
  v_bitop3_b32 v230, v72, v73, v74 bitop3:0x96
  v_bitop3_b32 v231, v76, v77, v78 bitop3:0x96
  v_add3_u32 v229, v229, v230, v231
  v_bitop3_b32 v230, v95, v75, v79 bitop3:0x96
  v_add3_u32 v229, v229, v230, s50
  v_cmp_ne_u32 vcc, v229, v29
  s_cbranch_vccnz .Lfb_n2_retry
  s_branch .Lfb_n2_ok
.Lfb_n2_give:
  s_mov_b32 s54, 0
  s_branch .Lfb_n2_ok
.Lfb_n3_retry:
  s_sub_u32 s54, s54, 1
  s_cbranch_scc1 .Lfb_n3_give
  s_sleep 1
  buffer_load_dwordx4 v[96:99], v2, s[36:39], s72 offen sc1
  s_mov_b64 exec, s[66:67]
  buffer_load_dwordx4 v[80:83], v4, s[36:39], s72 offen offset:1024 sc1
  buffer_load_dwordx4 v[84:87], v4, s[36:39], s72 offen offset:1536 sc1
  s_mov_b64 exec, -1
  buffer_load_dword v30, v3, s[36:39], s72 offen offset:2048 sc1
  s_waitcnt vmcnt(0)
  v_bitop3_b32 v229, v96, v97, v98 bitop3:0x96
  v_bitop3_b32 v230, v80, v81, v82 bitop3:0x96
  v_bitop3_b32 v231, v84, v85, v86 bitop3:0x96
  v_add3_u32 v229, v229, v230, v231
  v_bitop3_b32 v230, v99, v83, v87 bitop3:0x96
  v_add3_u32 v229, v229, v230, s50
  v_cmp_ne_u32 vcc, v229, v30
  s_cbranch_vccnz .Lfb_n3_retry
  s_branch .Lfb_n3_ok
.Lfb_n3_give:
  s_mov_b32 s54, 0
  s_branch .Lfb_n3_ok
.Lfb_n4_retry:
  s_sub_u32 s54, s54, 1
  s_cbranch_scc1 .Lfb_n4_give
  s_sleep 1
  buffer_load_dwordx4 v[36:39], v2, s[36:39], s73 offen sc1
  s_mov_b64 exec, s[66:67]
  buffer_load_dwordx4 v[56:59], v4, s[36:39], s73 offen offset:1024 sc1
  buffer_load_dwordx4 v[60:63], v4, s[36:39], s73 offen offset:1536 sc1
  s_mov_b64 exec, -1
  buffer_load_dword v31, v3, s[36:39], s73 offen offset:2048 sc1
  s_waitcnt vmcnt(0)
  v_bitop3_b32 v229, v36, v37, v38 bitop3:0x96
  v_bitop3_b32 v230, v56, v57, v58 bitop3:0x96
  v_bitop3_b32 v231, v60, v61, v62 bitop3:0x96
  v_add3_u32 v229, v229, v230, v231
  v_bitop3_b32 v230, v39, v59, v63 bitop3:0x96
  v_add3_u32 v229, v229, v230, s50
  v_cmp_ne_u32 vcc, v229, v31
  s_cbranch_vccnz .Lfb_n4_retry
  s_branch .Lfb_n4_ok
.Lfb_n4_give:
  s_mov_b32 s54, 0
  s_branch .Lfb_n4_ok
.Lfb_slow:
  s_waitcnt vmcnt(0)
  v_and_b32 v80, 27, v1
  v_mov_b32 v81, 1065369472
  v_cmp_eq_u32 vcc, 16, v80
  s_nop 1
  v_cndmask_b32_e64 v80, 0, v81, vcc
  v_mov_b32 v81, v80
  v_mov_b64 v[82:83], v[80:81]
  v_mov_b64 v[84:85], v[80:81]
  v_mov_b64 v[86:87], v[80:81]
  v_mov_b32 v92, 0
  v_mov_b32 v95, 0xff800000
  v_mov_b32 v93, v95
  s_lshl1_add_u32 s57, s47, s46
  s_min_u32 s57, s57, 8
  s_mov_b32 s58, 0
  s_movk_i32 s56, 0x100
.Lfb_slow_pass:
  v_mov_b64 v[188:189], 0
  v_mov_b64 v[190:191], 0
  v_mov_b64 v[192:193], 0
  v_mov_b64 v[194:195], 0
  v_mov_b64 v[196:197], 0
  v_mov_b64 v[198:199], 0
  v_mov_b64 v[200:201], 0
  v_mov_b64 v[202:203], 0
  s_mov_b32 s59, s57
.Lfb_slow_tile:
  s_sub_u32 s60, s59, s46
  s_and_b32 s68, s60, 1
  s_add_u32 s60, s60, 1
  s_lshr_b32 s60, s60, 1
  s_lshl_b32 s60, s60, 6
  s_add_u32 s60, s60, s46
  s_sub_u32 s60, s60, s68
  s_mul_i32 s60, s60, 2304
  s_sub_u32 s70, s53, s60
  buffer_load_dwordx4 v[88:91], v2, s[36:39], s70 offen sc1
  s_mov_b64 exec, s[66:67]
  buffer_load_dwordx4 v[80:83], v4, s[36:39], s70 offen offset:1024 sc1
  buffer_load_dwordx4 v[84:87], v4, s[36:39], s70 offen offset:1536 sc1
  s_mov_b64 exec, -1
  buffer_load_dword v28, v3, s[36:39], s70 offen offset:2048 sc1
  s_waitcnt vmcnt(0)
  v_mfma_f32_32x32x16_bf16 v[140:155], v[88:91], v[40:43], 0
  s_lshl_b32 s60, s59, 5
  s_sub_u32 s68, s60, 0
  v_add_u32 v94, s68, v16
  v_cmp_gt_u32 vcc, s56, v94
  s_nop 7
  v_cndmask_b32_e64 v140, v95, v140, vcc
  v_max_f32 v93, v93, v140
  v_sub_f32 v140, v140, v92
  v_exp_f32 v140, v140
  s_sub_u32 s68, s60, 1
  v_add_u32 v94, s68, v16
  v_cmp_gt_u32 vcc, s56, v94
  s_nop 1
  v_cndmask_b32_e64 v141, v95, v141, vcc
  v_max_f32 v93, v93, v141
  v_sub_f32 v141, v141, v92
  v_exp_f32 v141, v141
  s_sub_u32 s68, s60, 2
  v_add_u32 v94, s68, v16
  v_cmp_gt_u32 vcc, s56, v94
  s_nop 1
  v_cndmask_b32_e64 v142, v95, v142, vcc
  v_max_f32 v93, v93, v142
  v_sub_f32 v142, v142, v92
  v_exp_f32 v142, v142
  s_sub_u32 s68, s60, 3
  v_add_u32 v94, s68, v16
  v_cmp_gt_u32 vcc, s56, v94
  s_nop 1
  v_cndmask_b32_e64 v143, v95, v143, vcc
  v_max_f32 v93, v93, v143
  v_sub_f32 v143, v143, v92
  v_exp_f32 v143, v143
  s_sub_u32 s68, s60, 8
  v_add_u32 v94, s68, v16
  v_cmp_gt_u32 vcc, s56, v94
  s_nop 1
  v_cndmask_b32_e64 v144, v95, v144, vcc
  v_max_f32 v93, v93, v144
  v_sub_f32 v144, v144, v92
  v_exp_f32 v144, v144
  s_sub_u32 s68, s60, 9
  v_add_u32 v94, s68, v16
  v_cmp_gt_u32 vcc, s56, v94
  s_nop 1
  v_cndmask_b32_e64 v145, v95, v145, vcc
  v_max_f32 v93, v93, v145
  v_sub_f32 v145, v145, v92
  v_exp_f32 v145, v145
  s_sub_u32 s68, s60, 10
  v_add_u32 v94, s68, v16
  v_cmp_gt_u32 vcc, s56, v94
  s_nop 1
  v_cndmask_b32_e64 v146, v95, v146, vcc
  v_max_f32 v93, v93, v146
  v_sub_f32 v146, v146, v92
  v_exp_f32 v146, v146
  s_sub_u32 s68, s60, 11
  v_add_u32 v94, s68, v16
  v_cmp_gt_u32 vcc, s56, v94
  s_nop 1
  v_cndmask_b32_e64 v147, v95, v147, vcc
  v_max_f32 v93, v93, v147
  v_sub_f32 v147, v147, v92
  v_exp_f32 v147, v147
  s_sub_u32 s68, s60, 16
  v_add_u32 v94, s68, v16
  v_cmp_gt_u32 vcc, s56, v94
  s_nop 1
  v_cndmask_b32_e64 v148, v95, v148, vcc
  v_max_f32 v93, v93, v148
  v_sub_f32 v148, v148, v92
  v_exp_f32 v148, v148
  s_sub_u32 s68, s60, 17
  v_add_u32 v94, s68, v16
  v_cmp_gt_u32 vcc, s56, v94
  s_nop 1
  v_cndmask_b32_e64 v149, v95, v149, vcc
  v_max_f32 v93, v93, v149
  v_sub_f32 v149, v149, v92
  v_exp_f32 v149, v149
  s_sub_u32 s68, s60, 18
  v_add_u32 v94, s68, v16
  v_cmp_gt_u32 vcc, s56, v94
  s_nop 1
  v_cndmask_b32_e64 v150, v95, v150, vcc
  v_max_f32 v93, v93, v150
  v_sub_f32 v150, v150, v92
  v_exp_f32 v150, v150
  s_sub_u32 s68, s60, 19
  v_add_u32 v94, s68, v16
  v_cmp_gt_u32 vcc, s56, v94
  s_nop 1
  v_cndmask_b32_e64 v151, v95, v151, vcc
  v_max_f32 v93, v93, v151
  v_sub_f32 v151, v151, v92
  v_exp_f32 v151, v151
  s_sub_u32 s68, s60, 24
  v_add_u32 v94, s68, v16
  v_cmp_gt_u32 vcc, s56, v94
  s_nop 1
  v_cndmask_b32_e64 v152, v95, v152, vcc
  v_max_f32 v93, v93, v152
  v_sub_f32 v152, v152, v92
  v_exp_f32 v152, v152
  s_sub_u32 s68, s60, 25
  v_add_u32 v94, s68, v16
  v_cmp_gt_u32 vcc, s56, v94
  s_nop 1
  v_cndmask_b32_e64 v153, v95, v153, vcc
  v_max_f32 v93, v93, v153
  v_sub_f32 v153, v153, v92
  v_exp_f32 v153, v153
  s_sub_u32 s68, s60, 26
  v_add_u32 v94, s68, v16
  v_cmp_gt_u32 vcc, s56, v94
  s_nop 1
  v_cndmask_b32_e64 v154, v95, v154, vcc
  v_max_f32 v93, v93, v154
  v_sub_f32 v154, v154, v92
  v_exp_f32 v154, v154
  s_sub_u32 s68, s60, 27
  v_add_u32 v94, s68, v16
  v_cmp_gt_u32 vcc, s56, v94
  s_nop 1
  v_cndmask_b32_e64 v155, v95, v155, vcc
  v_max_f32 v93, v93, v155
  v_sub_f32 v155, v155, v92
  v_exp_f32 v155, v155
  v_cvt_pk_bf16_f32 v204, v140, v141
  v_cvt_pk_bf16_f32 v205, v142, v143
  v_cvt_pk_bf16_f32 v206, v144, v145
  v_cvt_pk_bf16_f32 v207, v146, v147
  v_cvt_pk_bf16_f32 v208, v148, v149
  v_cvt_pk_bf16_f32 v209, v150, v151
  v_cvt_pk_bf16_f32 v210, v152, v153
  v_cvt_pk_bf16_f32 v211, v154, v155
  v_mfma_f32_32x32x16_bf16 v[188:203], v[80:83], v[204:207], v[188:203]
  s_nop 0
  v_mfma_f32_32x32x16_bf16 v[188:203], v[84:87], v[208:211], v[188:203]
  s_nop 15
  s_sub_u32 s59, s59, 1
  s_cbranch_scc0 .Lfb_slow_tile
  v_mov_b32 v92, v93
  s_nop 1
  v_permlane32_swap_b32 v92, v93
  v_max_f32 v92, v92, v93
  s_xor_b32 s58, s58, 1
  s_cbranch_scc1 .Lfb_slow_pass
  v_rcp_f32 v230, v196
  v_subrev_u32 v229, s55, v196
  v_pk_mul_f32 v[188:189], v[188:189], v[230:231] op_sel_hi:[1,0]
  v_pk_mul_f32 v[190:191], v[190:191], v[230:231] op_sel_hi:[1,0]
  v_pk_mul_f32 v[192:193], v[192:193], v[230:231] op_sel_hi:[1,0]
  v_pk_mul_f32 v[194:195], v[194:195], v[230:231] op_sel_hi:[1,0]
  v_cvt_pk_bf16_f32 v220, v188, v189
  v_cvt_pk_bf16_f32 v221, v190, v191
  v_cvt_pk_bf16_f32 v222, v192, v193
  v_cvt_pk_bf16_f32 v223, v194, v195
  buffer_store_dwordx4 v[220:223], v10, s[36:39], s62 offen sc1
  s_branch .Lfb_slow_done
.Lfb_o0_retry:
  s_sub_u32 s54, s54, 1
  s_cbranch_scc1 .Lfb_o0_give
  buffer_load_dwordx4 v[4:7], v12, s[36:39], s70 offen sc1
  buffer_load_dwordx4 v[8:11], v12, s[36:39], s70 offen offset:1024 sc1
  buffer_load_dword v22, v13, s[36:39], s57 offen sc1
  s_waitcnt vmcnt(0)
  v_bitop3_b32 v198, v4, v5, v6 bitop3:0x96
  v_bitop3_b32 v199, v8, v9, v10 bitop3:0x96
  v_add3_u32 v198, v198, v7, v11
  v_add_u32 v198, v198, v199
  v_subrev_u32 v199, s50, v22
  s_nop 0
  v_add_u32_dpp v198, v198, v198 quad_perm:[1,0,3,2] row_mask:0xf bank_mask:0xf
  v_cmp_ne_u32 vcc, v198, v199
  s_cbranch_vccnz .Lfb_o0_retry
  s_branch .Lfb_o0_ok
.Lfb_o0_give:
  s_mov_b32 s54, 0
  s_branch .Lfb_o0_ok
.Lfb_o1_retry:
  s_sub_u32 s54, s54, 1
  s_cbranch_scc1 .Lfb_o1_give
  buffer_load_dwordx4 v[168:171], v12, s[36:39], s71 offen sc1
  buffer_load_dwordx4 v[172:175], v12, s[36:39], s71 offen offset:1024 sc1
  buffer_load_dword v23, v13, s[36:39], s58 offen sc1
  s_waitcnt vmcnt(0)
  v_bitop3_b32 v198, v168, v169, v170 bitop3:0x96
  v_bitop3_b32 v199, v172, v173, v174 bitop3:0x96
  v_add3_u32 v198, v198, v171, v175
  v_add_u32 v198, v198, v199
  v_subrev_u32 v199, s50, v23
  s_nop 0
  v_add_u32_dpp v198, v198, v198 quad_perm:[1,0,3,2] row_mask:0xf bank_mask:0xf
  v_cmp_ne_u32 vcc, v198, v199
  s_cbranch_vccnz .Lfb_o1_retry
  s_branch .Lfb_o1_ok
.Lfb_o1_give:
  s_mov_b32 s54, 0
  s_branch .Lfb_o1_ok
.Lfb_o2_retry:
  s_sub_u32 s54, s54, 1
  s_cbranch_scc1 .Lfb_o2_give
  buffer_load_dwordx4 v[176:179], v12, s[36:39], s72 offen sc1
  buffer_load_dwordx4 v[180:183], v12, s[36:39], s72 offen offset:1024 sc1
  buffer_load_dword v192, v13, s[36:39], s59 offen sc1
  s_waitcnt vmcnt(0)
  v_bitop3_b32 v198, v176, v177, v178 bitop3:0x96
  v_bitop3_b32 v199, v180, v181, v182 bitop3:0x96
  v_add3_u32 v198, v198, v179, v183
  v_add_u32 v198, v198, v199
  v_subrev_u32 v199, s50, v192
  s_nop 0
  v_add_u32_dpp v198, v198, v198 quad_perm:[1,0,3,2] row_mask:0xf bank_mask:0xf
  v_cmp_ne_u32 vcc, v198, v199
  s_cbranch_vccnz .Lfb_o2_retry
  s_branch .Lfb_o2_ok
.Lfb_o2_give:
  s_mov_b32 s54, 0
  s_branch .Lfb_o2_ok
.Lfb_o3_retry:
  s_sub_u32 s54, s54, 1
  s_cbranch_scc1 .Lfb_o3_give
  buffer_load_dwordx4 v[184:187], v12, s[36:39], s73 offen sc1
  buffer_load_dwordx4 v[188:191], v12, s[36:39], s73 offen offset:1024 sc1
  buffer_load_dword v193, v13, s[36:39], s60 offen sc1
  s_waitcnt vmcnt(0)
  v_bitop3_b32 v198, v184, v185, v186 bitop3:0x96
  v_bitop3_b32 v199, v188, v189, v190 bitop3:0x96
  v_add3_u32 v198, v198, v187, v191
  v_add_u32 v198, v198, v199
  v_subrev_u32 v199, s50, v193
  s_nop 0
  v_add_u32_dpp v198, v198, v198 quad_perm:[1,0,3,2] row_mask:0xf bank_mask:0xf
  v_cmp_ne_u32 vcc, v198, v199
  s_cbranch_vccnz .Lfb_o3_retry
  s_branch .Lfb_o3_ok
.Lfb_o3_give:
  s_mov_b32 s54, 0
  s_branch .Lfb_o3_ok
.Lfb_h3_retry:
  s_waitcnt lgkmcnt(0)
  s_sub_u32 s54, s54, 1
  s_cbranch_scc0 .Lfb_h3
  s_mov_b32 s54, 0
  s_branch .Lfb_h3_ok
.Lfb_plain:
  v_pk_add_f32 v[40:41], v[40:41], v[52:53]
  v_pk_add_f32 v[42:43], v[42:43], v[54:55]
  v_pk_add_f32 v[44:45], v[44:45], v[56:57]
  v_pk_add_f32 v[46:47], v[46:47], v[58:59]
  v_cvt_pk_bf16_f32 v74, v40, v41
  v_cvt_pk_bf16_f32 v75, v42, v43
  v_cvt_pk_bf16_f32 v76, v44, v45
  v_cvt_pk_bf16_f32 v77, v46, v47
  buffer_store_dwordx4 v[74:77], v31, s[32:35], s52 offen sc1
  s_branch .Lfb_exit
.Lfb_h4_retry:
  s_waitcnt lgkmcnt(0)
  s_sub_u32 s54, s54, 1
  s_cbranch_scc0 .Lfb_h4
  s_mov_b32 s54, 0
  s_branch .Lfb_h4_ok
.Lfb_h5_retry:
  s_waitcnt lgkmcnt(0)
  s_sub_u32 s54, s54, 1
  s_cbranch_scc0 .Lfb_h5
  s_mov_b32 s54, 0
  s_branch .Lfb_h5_ok
.Lfb_lose:
  s_mov_b64 exec, 0xffff
  buffer_store_dwordx2 v[156:157], v153, s[32:35], s52 offen sc1
  s_branch .Lfb_exit
.Lfb_h6_retry:
  s_waitcnt lgkmcnt(0)
  s_sub_u32 s54, s54, 1
  s_cbranch_scc0 .Lfb_h6
  s_mov_b32 s54, 0
  s_branch .Lfb_h6_ok
.Lfb_end:
)ASM"
               :: "{s[0:1]}"(__builtin_amdgcn_kernarg_segment_ptr()), "{s2}"(blockIdx.x), "{v0}"(threadIdx.x), "{s3}"((unsigned)(size_t)lds_)
               : "v1", "v2", "v3", "v4", "v5", "v6", "v7", "v8", "v9", "v10", "v11", "v12", "v13", "v14", "v15", "v16", "v17", "v18", "v19", "v20", "v21", "v22", "v23", "v24", "v25", "v26", "v27", "v28", "v29", "v30", "v31", "v32", "v33", "v34", "v35", "v36", "v37", "v38", "v39", "v40", "v41", "v42", "v43", "v44", "v45", "v46", "v47", "v48", "v49", "v50", "v51", "v52", "v53", "v54", "v55", "v56", "v57", "v58", "v59", "v60", "v61", "v62", "v63", "v64", "v65", "v66", "v67", "v68", "v69", "v70", "v71", "v72", "v73", "v74", "v75", "v76", "v77", "v78", "v79", "v80", "v81", "v82", "v83", "v84", "v85", "v86", "v87", "v88", "v89", "v90", "v91", "v92", "v93", "v94", "v95", "v96", "v97", "v98", "v99", "v100", "v101", "v102", "v103", "v104", "v105", "v106", "v107", "v108", "v109", "v110", "v111", "v112", "v113", "v114", "v115", "v116", "v117", "v118", "v119", "v120", "v121", "v122", "v123", "v124", "v125", "v126", "v127", "v128", "v129", "v130", "v131", "v132", "v133", "v134", "v135", "v136", "v137", "v138", "v139", "v140", "v141", "v142", "v143", "v144", "v145", "v146", "v147", "v148", "v149", "v150", "v151", "v152", "v153", "v154", "v155", "v156", "v157", "v158", "v159", "v160", "v161", "v162", "v163", "v164", "v165", "v166", "v167", "v168", "v169", "v170", "v171", "v172", "v173", "v174", "v175", "v176", "v177", "v178", "v179", "v180", "v181", "v182", "v183", "v184", "v185", "v186", "v187", "v188", "v189", "v190", "v191", "v192", "v193", "v194", "v195", "v196", "v197", "v198", "v199", "v200", "v201", "v202", "v203", "v204", "v205", "v206", "v207", "v208", "v209", "v210", "v211", "v212", "v213", "v214", "v215", "v216", "v217", "v218", "v219", "v220", "v221", "v222", "v223", "v224", "v225", "v226", "v227", "v228", "v229", "v230", "v231", "v232", "v233", "v234", "v235", "v236", "v237", "v238", "v239", "s4", "s5", "s6", "s7", "s8", "s9", "s10", "s11", "s12", "s13", "s14", "s15", "s16", "s17", "s18", "s19", "s20", "s21", "s22", "s23", "s24", "s25", "s26", "s27", "s28", "s29", "s30", "s31", "s33", "s34", "s35", "s36", "s37", "s38", "s39", "s40", "s41", "s42", "s43", "s44", "s45", "s46", "s47", "s48", "s49", "s50", "s51", "s52", "s53", "s54", "s55", "s56", "s57", "s58", "s59", "s60", "s61", "s62", "s63", "s64", "s65", "s66", "s67", "s68", "s69", "s70", "s71", "s72", "s73", "s74", "s75", "s76", "vcc", "scc", "memory");
#endif
}

extern "C" void kernel_launch(void* const* d_in, const int* in_sizes, int n_in, void* d_out, int out_size, void* d_ws, size_t ws_size,
                              hipStream_t stream) {
  fused_block<<<256, 512, 0, stream>>>((const u16*)d_in[0], (const u16*)d_in[1], (const u16*)d_in[2], (const u16*)d_in[4], (const u16*)d_in[5],
                                       (const u16*)d_in[6], (const u16*)d_in[7], (u16*)d_out, (char*)d_ws + WS_BASE(ws_size));
}
```
